```python
import torch

def _softmax(x, axis=-1):
    return torch.softmax(x, dim=axis)
def _gelu(x):
    return torch.nn.functional.gelu(x, approximate="tanh")
def _silu(x):
    return torch.nn.functional.silu(x)
def _sigmoid(x):
    return torch.sigmoid(x)
def _layernorm(x, axis=-1, eps=1e-5):
    m = x.mean(dim=axis, keepdim=True)
    v = x.var(dim=axis, keepdim=True, unbiased=False)
    return (x - m) / torch.sqrt(v + eps)
def _scatter_add(tgt, idx, src, axis=0):
    return tgt.clone().index_add_(axis, idx, src)
def _segment_sum(src, seg, n):
    out = torch.zeros((n,) + src.shape[1:], dtype=src.dtype)
    return out.index_add_(0, seg, src)
def _pad(x, pw):
    flat = tuple(v for pair in reversed(pw) for v in pair)
    return torch.nn.functional.pad(x, flat)

kernel_name = 'standalone__edge_featured_mpnn_egc__graph'

def setup_inputs():

    _w = (lambda a: (lambda t: t.to(torch.bfloat16) if t.is_floating_point() else t)(torch.as_tensor(a)))
    N, deg, D, De = 12500, 32, 128, max(16, 1)
    E = N * deg
    return {
        "x": _w((lambda *s: torch.randn(*s, dtype=torch.bfloat16) * 0.1)(N, D)),
        "e_attr": _w((lambda *s: torch.randn(*s, dtype=torch.bfloat16) * 0.1)(E, De)),
        "src": _w((lambda lo, hi, *s: torch.randint(lo, hi, s))(0, N, E).to(torch.int64)),
        "dst": _w((lambda lo, hi, *s: torch.randint(lo, hi, s))(0, N, E).to(torch.int64)),
        "u_w": _w((lambda *s: torch.randn(*s, dtype=torch.bfloat16) * 0.05)(D, D)),
        "v_w": _w((lambda *s: torch.randn(*s, dtype=torch.bfloat16) * 0.05)(D, D)),
        "g_w": _w((lambda *s: torch.randn(*s, dtype=torch.bfloat16) * 0.1)(2 * D + De, D)),
    }

def reference(x, e_attr, src, dst, u_w, v_w, g_w):
    N, D = x.shape
    g_in = torch.concatenate([x[dst], x[src], e_attr], axis=-1)
    eta = _sigmoid(g_in @ g_w)
    hv = x @ v_w
    msg = eta * hv[src]
    agg = _segment_sum(msg, dst, N)
    return _silu(x @ u_w + agg)
```

```cpp
#include <hip/hip_runtime.h>
#include <hip/hip_bf16.h>

typedef unsigned u32;
typedef unsigned short u16;
typedef u32 u32x4 __attribute__((ext_vector_type(4)));
typedef u32 u32x2 __attribute__((ext_vector_type(2)));
typedef short bf16x8 __attribute__((ext_vector_type(8)));
typedef float f32x16 __attribute__((ext_vector_type(16)));
typedef float f32x2 __attribute__((ext_vector_type(2)));
typedef float f32x4 __attribute__((ext_vector_type(4)));

namespace {

constexpr int N_NODES = 12500, N_EDGES = 400000, DM = 128, DE = 16;
constexpr int BN = 25;
constexpr int NB = N_NODES / BN;
constexpr int NCH = 100;
constexpr int CH = N_EDGES / NCH;
constexpr int TROWS = N_NODES + 1;
constexpr float NLOG2E = -1.4426950408889634f;

constexpr size_t WS_T = 1024;
constexpr size_t T_BLK = (size_t)TROWS * 128;
constexpr size_t WS_XA = (WS_T + 4 * T_BLK + 1023) / 1024 * 1024;
constexpr size_t WS_XU = WS_XA + (size_t)NB * 8192;
constexpr size_t WS_REC = WS_XU + (size_t)NB * 8192;
constexpr size_t WS_SEG = WS_REC + (size_t)N_EDGES * 4;
constexpr size_t WS_END = WS_SEG + (size_t)NB * NCH * 4;
static_assert(WS_XA % 256 == 0, "align");

constexpr int G1_GEMM = 252;
constexpr int G1 = G1_GEMM + NCH;

__device__ __forceinline__ u32 cvt_pk_bf16(float lo, float hi) {
  u32 r;
  asm("v_cvt_pk_bf16_f32 %0, %1, %2" : "=v"(r) : "v"(lo), "v"(hi));
  return r;
}
__device__ __forceinline__ f32x16 mfma32(bf16x8 a, bf16x8 b, f32x16 c) {
  return __builtin_amdgcn_mfma_f32_32x32x16_bf16(a, b, c, 0, 0, 0);
}
__device__ __forceinline__ bf16x8 as_frag(u32x4 v) { return __builtin_bit_cast(bf16x8, v); }

constexpr int XPITCH = 272;
constexpr int K1_LDS = 8 * BN * XPITCH + XPITCH;

__global__ __launch_bounds__(512, 4) void egc_prep(const u16* __restrict__ x, const int* __restrict__ src,
                                                   const int* __restrict__ dst, const u16* __restrict__ uw,
                                                   const u16* __restrict__ vw, const u16* __restrict__ gw,
                                                   char* __restrict__ ws) {
  __shared__ __attribute__((aligned(16))) char lds[K1_LDS];
  const int tid = threadIdx.x, lane = tid & 63, wave = __builtin_amdgcn_readfirstlane(tid >> 6);
  const int c = lane & 31, hk = lane >> 5;

  if (blockIdx.x < G1_GEMM) {
    const int gp = blockIdx.x & 3, g = gp & 1, pair = gp >> 1;
    const int b = (blockIdx.x >> 2) * 8 + wave;
    const bool live = b < NB;
    const u16* m0 = pair == 0 ? gw + 128 * DM : gw;
    const u16* m1 = pair == 0 ? vw : uw;
    u32 wr[2][8];
#pragma unroll
    for (int i = 0; i < 8; i++) {
      const int row = 16 * wave + 8 * hk + i;
      wr[0][i] = *(const u32*)(m0 + row * DM + 64 * g + 2 * c);
      wr[1][i] = *(const u32*)(m1 + row * DM + 64 * g + 2 * c);
    }
    u32x4 xv[7];
    const char* xb = (const char*)x + (size_t)(live ? b : 0) * (BN * 256);
#pragma unroll
    for (int q = 0; q < 7; q++) {
      const int ci = q * 64 + lane;
      xv[q] = (ci < BN * 16) ? *(const u32x4*)(xb + ci * 16) : u32x4{0, 0, 0, 0};
    }
    char* xt = lds + wave * (BN * XPITCH);
    char* zrow = lds + 8 * BN * XPITCH;
    if (tid < 17) *(u32x4*)(zrow + tid * 16) = u32x4{0, 0, 0, 0};
#pragma unroll
    for (int q = 0; q < 7; q++) {
      const int ci = q * 64 + lane;
      if (ci < BN * 16) *(u32x4*)(xt + (ci >> 4) * XPITCH + (ci & 15) * 16) = xv[q];
    }
    __syncthreads();
    bf16x8 af[8];
    {
      const char* rp = (c < BN ? xt + c * XPITCH : zrow) + hk * 16;
#pragma unroll
      for (int s = 0; s < 8; s++) af[s] = *(const bf16x8*)(rp + s * 32);
    }
    __syncthreads();
#pragma unroll
    for (int m = 0; m < 2; m++) {
      u32x4 fe, fo;
#pragma unroll
      for (int i = 0; i < 4; i++) {
        fe[i] = __builtin_amdgcn_perm(wr[m][2 * i + 1], wr[m][2 * i], 0x05040100u);
        fo[i] = __builtin_amdgcn_perm(wr[m][2 * i + 1], wr[m][2 * i], 0x07060302u);
      }
      *(u32x4*)(lds + ((wave * 4 + m * 2 + 0) * 64 + lane) * 16) = fe;
      *(u32x4*)(lds + ((wave * 4 + m * 2 + 1) * 64 + lane) * 16) = fo;
    }
    __syncthreads();
    f32x16 acc[4];
#pragma unroll
    for (int a = 0; a < 4; a++)
#pragma unroll
      for (int r = 0; r < 16; r++) acc[a][r] = 0.f;
#pragma unroll
    for (int s = 0; s < 8; s++) {
#pragma unroll
      for (int a = 0; a < 4; a++) {
        const bf16x8 bf = *(const bf16x8*)(lds + ((s * 4 + a) * 64 + lane) * 16);
        acc[a] = mfma32(af[s], bf, acc[a]);
      }
    }
    if (!live) return;
    if (pair == 0) {
#pragma unroll
      for (int p = 0; p < 2; p++) {
        char* tb = ws + WS_T + (size_t)(2 * g + p) * T_BLK + (size_t)b * (BN * 128) + c * 4;
#pragma unroll
        for (int r = 0; r < 13; r++) {
          const int row = (r & 3) + 8 * (r >> 2) + 4 * hk;
          const u32 lo = cvt_pk_bf16(acc[2 + p][r], 0.f) & 0xffffu;
          const u32 X = __float_as_uint(acc[p][r] * NLOG2E);
          u32 M = X & 0x7fffffffu;
          M = M > 0x7f7f0000u ? 0x7f7f0000u : M;
          int t = (int)(M + 0x8000u) - (int)lo;
          t = t < 0 ? 0 : t;
          const u32 P = (X & 0x80000000u) | ((u32)t & 0x7fff0000u) | lo;
          if (row < BN) *(u32*)(tb + row * 128) = P;
        }
      }
      if (b == 0 && hk == 0) {
        *(u32*)(ws + WS_T + (size_t)(2 * g) * T_BLK + (size_t)N_NODES * 128 + c * 4) = 0u;
        *(u32*)(ws + WS_T + (size_t)(2 * g + 1) * T_BLK + (size_t)N_NODES * 128 + c * 4) = 0u;
      }
    } else {
#pragma unroll
      for (int p = 0; p < 2; p++)
#pragma unroll
        for (int s = 0; s < 2; s++) {
          u32x4 f;
#pragma unroll
          for (int i = 0; i < 4; i++)
            f[i] = cvt_pk_bf16(acc[p][8 * s + 2 * i] * NLOG2E, acc[p][8 * s + 2 * i + 1] * NLOG2E);
          *(u32x4*)(ws + WS_XA + (size_t)b * 8192 + (((2 * g + p) * 2 + s) * 64 + lane) * 16) = f;
        }
#pragma unroll
      for (int q = 0; q < 4; q++) {
        u32x4 f;
#pragma unroll
        for (int i = 0; i < 4; i++) f[i] = cvt_pk_bf16(acc[2][4 * q + i], acc[3][4 * q + i]);
        *(u32x4*)(ws + WS_XU + (size_t)b * 8192 + ((g * 4 + q) * 64 + lane) * 16) = f;
      }
    }
    return;
  }

  const int chunk = blockIdx.x - G1_GEMM;
  u32* cnt = (u32*)lds;
  u32* sorted = (u32*)(lds + 16384);
  u32* bstart = (u32*)(lds + 32768);
  u32* wsum = (u32*)(lds + 32768 + 2048);
  const bool act = tid < 500;
  int4 dv[2], sv[2];
#pragma unroll
  for (int q = 0; q < 2; q++) {
    const int i4 = act ? tid + 500 * q : 0;
    dv[q] = ((const int4*)(dst + chunk * CH))[i4];
    sv[q] = ((const int4*)(src + chunk * CH))[i4];
  }
  *(u32x4*)(lds + tid * 32) = u32x4{0, 0, 0, 0};
  *(u32x4*)(lds + tid * 32 + 16) = u32x4{0, 0, 0, 0};
  __syncthreads();
  u32 bk[8], rk[8], rec[8];
#pragma unroll
  for (int e = 0; e < 8; e++) {
    const int q = e >> 2, i = e & 3;
    const u32 d = (u32)(i == 0 ? dv[q].x : i == 1 ? dv[q].y : i == 2 ? dv[q].z : dv[q].w);
    const u32 s = (u32)(i == 0 ? sv[q].x : i == 1 ? sv[q].y : i == 2 ? sv[q].z : sv[q].w);
    bk[e] = (d * 5243u) >> 17;
    rec[e] = s | ((d - bk[e] * BN) << 14) | ((u32)((tid + 500 * q) * 4 + i) << 19);
    rk[e] = act ? atomicAdd(&cnt[wave * 512 + bk[e]], 1u) : 0u;
  }
  __syncthreads();
  u32 tot = 0;
#pragma unroll
  for (int w = 0; w < 8; w++) {
    const u32 v = cnt[w * 512 + tid];
    cnt[w * 512 + tid] = tot;
    tot += v;
  }
  u32 inc = tot;
#pragma unroll
  for (int o = 1; o < 64; o <<= 1) {
    const u32 v = __shfl_up(inc, o, 64);
    if (lane >= o) inc += v;
  }
  if (lane == 63) wsum[wave] = inc;
  __syncthreads();
  u32 wb = 0;
#pragma unroll
  for (int w = 0; w < 8; w++) wb += (w < wave) ? wsum[w] : 0u;
  const u32 start = wb + inc - tot;
  bstart[tid] = start;
  if (tid < NB) ((u32*)(ws + WS_SEG))[tid * NCH + chunk] = start | (tot << 16);
  __syncthreads();
  if (act) {
#pragma unroll
    for (int e = 0; e < 8; e++) sorted[bstart[bk[e]] + cnt[wave * 512 + bk[e]] + rk[e]] = rec[e];
  }
  __syncthreads();
  if (act) {
    u32x4* o = (u32x4*)(ws + WS_REC + (size_t)chunk * CH * 4);
    o[tid] = ((const u32x4*)sorted)[tid];
    o[tid + 500] = ((const u32x4*)sorted)[tid + 500];
  }
}

constexpr int NW = 4;
constexpr int NT = NW * 64;
constexpr int CAP = 1024;
constexpr int IPITCH = 80;
constexpr int IMG = 33 * IPITCH;
constexpr int L_IMG = 0;
constexpr int L_SO = L_IMG + NW * 2 * IMG;
constexpr int L_EO = L_SO + CAP * 4;
constexpr int L_SA = L_EO + CAP * 4;
constexpr int L_MISC = L_SA + CAP * 4;
constexpr int L_END0 = L_MISC + 64;
constexpr int L_RED = NW * 2 * 4096;
constexpr int K2_LDS = L_END0 > L_RED ? L_END0 : L_RED;
static_assert(L_SO % 16 == 0, "align");

__global__ __launch_bounds__(NT, 2) void egc_edge(const u16* __restrict__ eattr, const u16* __restrict__ gw,
                                                   const char* __restrict__ ws, u16* __restrict__ out) {
  __shared__ __attribute__((aligned(16))) char lds[K2_LDS];
  const int tid = threadIdx.x, lane = tid & 63, wave = __builtin_amdgcn_readfirstlane(tid >> 6);
  const int c = lane & 31, hk = lane >> 5;
  const int xr = blockIdx.x & 7, g = xr & 1;
  const int b = (blockIdx.x >> 3) * 4 + (xr >> 1);

  u32 sv = 0;
  if (tid < NCH) sv = ((const u32*)(ws + WS_SEG))[b * NCH + tid];
  bf16x8 xaf[2][2];
#pragma unroll
  for (int p = 0; p < 2; p++)
#pragma unroll
    for (int s = 0; s < 2; s++)
      xaf[p][s] = *(const bf16x8*)(ws + WS_XA + (size_t)b * 8192 + (((2 * g + p) * 2 + s) * 64 + lane) * 16);
  u32 cr[8];
#pragma unroll
  for (int i = 0; i < 8; i++) cr[i] = *(const u32*)(gw + (256 + 8 * hk + i) * DM + 64 * g + 2 * c);
  for (int i = tid; i < NW * 2 * IMG / 16; i += NT) *(u32x4*)(lds + L_IMG + i * 16) = u32x4{0, 0, 0, 0};

  const u32 scnt = sv >> 16, sstart = sv & 0xffffu;
  u32 inc = scnt;
#pragma unroll
  for (int o = 1; o < 64; o <<= 1) {
    const u32 v = __shfl_up(inc, o, 64);
    if (lane >= o) inc += v;
  }
  u32* misc = (u32*)(lds + L_MISC);
  if (lane == 63 && wave < 2) misc[wave] = inc;
  __syncthreads();
  const u32 w0 = misc[0], w1 = misc[1];
  const int M = __builtin_amdgcn_readfirstlane((int)(w0 + w1));
  const int pre = (int)(inc - scnt + (wave == 1 ? w0 : 0u));

  bf16x8 chi[2], clo[2];
#pragma unroll
  for (int p = 0; p < 2; p++) {
    u32x4 h4, l4;
#pragma unroll
    for (int i = 0; i < 4; i++) {
      const float v0 = __uint_as_float(p ? (cr[2 * i] & 0xffff0000u) : (cr[2 * i] << 16)) * NLOG2E;
      const float v1 = __uint_as_float(p ? (cr[2 * i + 1] & 0xffff0000u) : (cr[2 * i + 1] << 16)) * NLOG2E;
      const u32 h = cvt_pk_bf16(v0, v1);
      h4[i] = h;
      l4[i] = cvt_pk_bf16(v0 - __uint_as_float(h << 16), v1 - __uint_as_float(h & 0xffff0000u));
    }
    chi[p] = as_frag(h4);
    clo[p] = as_frag(l4);
  }

  const auto trs = __builtin_amdgcn_make_buffer_rsrc((void*)(ws + WS_T - 128), 4, 0x7fffffff, 1 << 23);
  const auto ers = __builtin_amdgcn_make_buffer_rsrc((void*)eattr, 0, N_EDGES * 32, 0x00020000);
  const int soff0 = (int)((2 * g) * T_BLK), soff1 = (int)((2 * g + 1) * T_BLK);
  const u32* rec = (const u32*)(ws + WS_REC);
  u32* so = (u32*)(lds + L_SO);
  u32* eo = (u32*)(lds + L_EO);
  u32* sa = (u32*)(lds + L_SA);
  char* img = lds + L_IMG + wave * 2 * IMG;
  const int pm = ((c >> 2) & 1) * 16 + (c & 3) + 4 * (c >> 3);

  f32x16 acc[2];
#pragma unroll
  for (int r = 0; r < 16; r++) acc[0][r] = acc[1][r] = 0.f;

  for (int base = 0; base < M; base += CAP) {
    if (base) __syncthreads();
    {
      int i0 = base - pre, i1 = base + CAP - pre;
      i0 = i0 < 0 ? 0 : i0;
      i1 = i1 > (int)scnt ? (int)scnt : i1;
      for (int i = i0; i < i1; i++) sa[pre + i - base] = (u32)(tid * CH) + sstart + (u32)i;
    }
    __syncthreads();
    const int m = M - base < CAP ? M - base : CAP;
    const int nt = (m + 31) >> 5;
    u32 ri[CAP / NT], rv[CAP / NT];
#pragma unroll
    for (int k = 0; k < CAP / NT; k++) {
      const int p = tid + k * NT;
      ri[k] = p < m ? sa[p] : 0u;
      rv[k] = rec[ri[k]];
    }
#pragma unroll
    for (int k = 0; k < CAP / NT; k++) {
      const int p = tid + k * NT;
      if (p < nt * 32) {
        const int h = (p >> 4) & 1, i = p & 15;
        const int mrow = (i & 3) + 8 * (i >> 2) + 4 * h;
        const bool ok = p < m;
        const u32 s = ok ? (rv[k] & 0x3fffu) : (u32)N_NODES;
        const u32 d = (rv[k] >> 14) & 31u;
        const u32 eid = (ri[k] / (u32)CH) * (u32)CH + (rv[k] >> 19);
        so[p] = s * 128u + 128u - (u32)h * 128u;
        eo[p] = ok ? eid * 32u : 0x40000000u;
        const u32 a2 = d * IPITCH + (i >> 3) * 32 + h * 16 + (i & 7) * 2;
        const u32 a1 = IMG + mrow * IPITCH + (d >> 4) * 32 + ((d >> 2) & 1) * 16 + (((d >> 3) & 1) * 4 + (d & 3)) * 2;
        sa[p] = ok ? (a2 | (a1 << 16)) : ((32u * IPITCH) | ((u32)(IMG + 32 * IPITCH) << 16));
      }
    }
    __syncthreads();

    for (int t = wave; t < nt; t += NW) {
      u32x4 o4[4];
#pragma unroll
      for (int q = 0; q < 4; q++) o4[q] = *(const u32x4*)(so + t * 32 + hk * 16 + q * 4);
      const u32 eoff = eo[t * 32 + pm] + hk * 16;
      const u32 sav = sa[t * 32 + c];
      f32x16 L[2];
#pragma unroll
      for (int i = 0; i < 16; i++) L[0][i] = __uint_as_float(__builtin_amdgcn_raw_buffer_load_b32(trs, o4[i >> 2][i & 3], soff0, 0));
      const bf16x8 ea = as_frag(__builtin_amdgcn_raw_buffer_load_b128(ers, eoff, 0, 0));
#pragma unroll
      for (int i = 0; i < 16; i++) L[1][i] = __uint_as_float(__builtin_amdgcn_raw_buffer_load_b32(trs, o4[i >> 2][i & 3], soff1, 0));
      u16* sp = (u16*)(img + (hk ? (sav >> 16) : (sav & 0xffffu)));
      *sp = (u16)0x3f80;
      bf16x8 sf[2], of[2];
#pragma unroll
      for (int s = 0; s < 2; s++) {
        sf[s] = *(const bf16x8*)(img + c * IPITCH + s * 32 + hk * 16);
        of[s] = *(const bf16x8*)(img + IMG + c * IPITCH + s * 32 + hk * 16);
      }
      *sp = (u16)0;
#pragma unroll
      for (int p = 0; p < 2; p++) {
        f32x16 z = mfma32(ea, chi[p], L[p]);
        z = mfma32(ea, clo[p], z);
        z = mfma32(of[0], xaf[p][0], z);
        z = mfma32(of[1], xaf[p][1], z);
        u32x4 mb[2];
#pragma unroll
        for (int i = 0; i < 16; i += 2) {
          f32x2 e = {__builtin_amdgcn_exp2f(z[i]), __builtin_amdgcn_exp2f(z[i + 1])};
          e = e + f32x2{1.f, 1.f};
          const f32x2 r = {__builtin_amdgcn_rcpf(e[0]), __builtin_amdgcn_rcpf(e[1])};
          const f32x2 hv = {__uint_as_float(__float_as_uint(L[p][i]) << 16), __uint_as_float(__float_as_uint(L[p][i + 1]) << 16)};
          const f32x2 mm = hv * r;
          mb[i >> 3][(i >> 1) & 3] = cvt_pk_bf16(mm[0], mm[1]);
        }
        acc[p] = mfma32(sf[0], as_frag(mb[0]), acc[p]);
        acc[p] = mfma32(sf[1], as_frag(mb[1]), acc[p]);
      }
    }
  }

  u32x4 xu[4 / NW > 0 ? 4 / NW : 1];
  static_assert(NW == 4, "epilogue assumes 4 waves");
  xu[0] = *(const u32x4*)(ws + WS_XU + (size_t)b * 8192 + ((g * 4 + wave) * 64 + lane) * 16);
  __syncthreads();
#pragma unroll
  for (int p = 0; p < 2; p++)
#pragma unroll
    for (int q = 0; q < 4; q++)
      *(f32x4*)(lds + (((wave * 2 + p) * 4 + q) * 64 + lane) * 16) =
          f32x4{acc[p][4 * q], acc[p][4 * q + 1], acc[p][4 * q + 2], acc[p][4 * q + 3]};
  __syncthreads();
  f32x4 y[2];
#pragma unroll
  for (int p = 0; p < 2; p++) {
    y[p] = *(const f32x4*)(lds + (((0 * 2 + p) * 4 + wave) * 64 + lane) * 16);
#pragma unroll
    for (int w = 1; w < NW; w++) y[p] += *(const f32x4*)(lds + (((w * 2 + p) * 4 + wave) * 64 + lane) * 16);
  }
#pragma unroll
  for (int i = 0; i < 4; i++) {
    const int row = i + 8 * wave + 4 * hk;
    const float y0 = y[0][i] + __uint_as_float(xu[0][i] << 16);
    const float y1 = y[1][i] + __uint_as_float(xu[0][i] & 0xffff0000u);
    const float o0 = y0 * __builtin_amdgcn_rcpf(1.f + __builtin_amdgcn_exp2f(y0 * NLOG2E));
    const float o1 = y1 * __builtin_amdgcn_rcpf(1.f + __builtin_amdgcn_exp2f(y1 * NLOG2E));
    if (row < BN) *(u32*)(out + (size_t)(b * BN + row) * DM + 64 * g + 2 * c) = cvt_pk_bf16(o0, o1);
  }
}

}

extern "C" void kernel_launch(void* const* d_in, const int* in_sizes, int n_in, void* d_out, int out_size,
                              void* d_ws, size_t ws_size, hipStream_t stream) {
  const u16* x = (const u16*)d_in[0];
  const u16* ea = (const u16*)d_in[1];
  const int* src = (const int*)d_in[2];
  const int* dst = (const int*)d_in[3];
  const u16* uw = (const u16*)d_in[4];
  const u16* vw = (const u16*)d_in[5];
  const u16* gw = (const u16*)d_in[6];
  egc_prep<<<G1, 512, 0, stream>>>(x, src, dst, uw, vw, gw, (char*)d_ws);
  egc_edge<<<2 * NB, NT, 0, stream>>>(ea, gw, (const char*)d_ws, (u16*)d_out);
}
```

```cpp
#include <hip/hip_runtime.h>
#include <hip/hip_bf16.h>

typedef unsigned u32;
typedef unsigned short u16;
typedef u32 u32x4 __attribute__((ext_vector_type(4)));
typedef u32 u32x2 __attribute__((ext_vector_type(2)));
typedef short bf16x8 __attribute__((ext_vector_type(8)));
typedef float f32x16 __attribute__((ext_vector_type(16)));
typedef float f32x2 __attribute__((ext_vector_type(2)));
typedef float f32x4 __attribute__((ext_vector_type(4)));

namespace {

constexpr int N_NODES = 12500, N_EDGES = 400000, DM = 128, DE = 16;
constexpr int BN = 25;
constexpr int NB = N_NODES / BN;
constexpr int NSUB = 4;
constexpr int NSB = NB / NSUB;
constexpr int NCH = 250;
constexpr int CH = N_EDGES / NCH;
constexpr int SLOT = 32;
constexpr int SCAP = SLOT;
constexpr int TROWS = N_NODES + 1;
constexpr float NLOG2E = -1.4426950408889634f;
__host__ __device__ constexpr u32 rec_dbits(u32 d) {
  return (((d & 0x13u) | ((d & 4u) << 1) | ((d & 8u) >> 1)) << 1) | (d << 22) | (((d >> 2) & 3u) << 20);
}
constexpr u32 REC_NONE = 0xffffffffu;
constexpr u32 REC_PAD = (u32)N_NODES << 6 | rec_dbits(31);

constexpr size_t WS_T = 1024;
constexpr size_t T_BLK = (size_t)TROWS * 128;
constexpr size_t WS_XA = (WS_T + 4 * T_BLK + 1023) / 1024 * 1024;
constexpr size_t WS_XU = WS_XA + (size_t)NB * 8192;
constexpr size_t WS_SLOT = WS_XU + (size_t)NB * 8192;
constexpr size_t WS_SPN = WS_SLOT + (size_t)NSB * NCH * SLOT * 4;
constexpr size_t WS_SPILL = WS_SPN + 1024;
constexpr size_t WS_EC = WS_SPILL + (size_t)N_EDGES * 8;
constexpr size_t WS_CF = WS_EC + (size_t)N_EDGES * 32;
constexpr size_t WS_HT = WS_CF + 8192;
constexpr size_t WS_END = WS_HT + (size_t)NSB * 2048;

constexpr int G1_GEMM = 256;
constexpr int G1 = G1_GEMM + NCH;

__device__ __forceinline__ u32 cvt_pk_bf16(float lo, float hi) {
  u32 r;
  asm("v_cvt_pk_bf16_f32 %0, %1, %2" : "=v"(r) : "v"(lo), "v"(hi));
  return r;
}
__device__ __forceinline__ f32x16 mfma32(bf16x8 a, bf16x8 b, f32x16 c) {
  return __builtin_amdgcn_mfma_f32_32x32x16_bf16(a, b, c, 0, 0, 0);
}
__device__ __forceinline__ int fresh_lane() {
  int l;
  asm volatile("v_mbcnt_lo_u32_b32 %0, -1, 0\n\tv_mbcnt_hi_u32_b32 %0, -1, %0" : "=v"(l));
  return l;
}
__device__ __forceinline__ u32 wave_scan(u32 v) {
  v += (u32)__builtin_amdgcn_update_dpp(0, (int)v, 0x111, 0xf, 0xf, false);
  v += (u32)__builtin_amdgcn_update_dpp(0, (int)v, 0x112, 0xf, 0xf, false);
  v += (u32)__builtin_amdgcn_update_dpp(0, (int)v, 0x114, 0xf, 0xf, false);
  v += (u32)__builtin_amdgcn_update_dpp(0, (int)v, 0x118, 0xf, 0xf, false);
  v += (u32)__builtin_amdgcn_update_dpp(0, (int)v, 0x142, 0xa, 0xf, false);
  v += (u32)__builtin_amdgcn_update_dpp(0, (int)v, 0x143, 0xc, 0xf, false);
  return v;
}
__device__ __forceinline__ void st16(char* ws, size_t off, u32x4 v) {
  const auto rs = __builtin_amdgcn_make_buffer_rsrc((void*)ws, 0, 0x7fffffff, 0x00020000);
  __builtin_amdgcn_raw_buffer_store_b128(v, rs, (int)off, 0,   16);
}
__device__ __forceinline__ bf16x8 as_frag(u32x4 v) { return __builtin_bit_cast(bf16x8, v); }

constexpr int XPITCH = 272;
constexpr int K1_STG = 0;
constexpr int K1_IMG = CH * 32;
constexpr int K1_DPOS = K1_IMG + NSB * SLOT * 4;
constexpr int K1_BST = K1_DPOS + CH * 2;
constexpr int K1_WS = K1_BST + 2064;
constexpr int K1_LDS = K1_WS + 64;
static_assert(8 * BN * XPITCH + XPITCH <= K1_LDS && K1_LDS <= 81920, "lds");

__global__ __launch_bounds__(512, 4) void egc_prep(const u16* __restrict__ x, const u16* __restrict__ eattr,
                                                   const int* __restrict__ src,
                                                   const int* __restrict__ dst, const u16* __restrict__ uw,
                                                   const u16* __restrict__ vw, const u16* __restrict__ gw,
                                                   char* __restrict__ ws) {
  __shared__ __attribute__((aligned(16))) char lds[K1_LDS];
  const int tid = threadIdx.x, lane = tid & 63, wave = __builtin_amdgcn_readfirstlane(tid >> 6);
  const int c = lane & 31, hk = lane >> 5;

  const bool is_gemm = blockIdx.x < G1_GEMM;
  const int ridx = is_gemm ? blockIdx.x : blockIdx.x - G1_GEMM;
  if (is_gemm) {
    const int bi = ridx;
    const int gp = (bi >> 3) & 3, g = gp & 1, pair = gp >> 1;
    const int b = ((bi >> 5) * 8 + (bi & 7)) * 8 + wave;
    const bool live = b < NB;
    if (b >= NB + 4) {
      if (pair == 0 && wave == 7) {
        u32 cr[8];
#pragma unroll
        for (int i = 0; i < 8; i++) cr[i] = *(const u32*)(gw + (256 + 8 * hk + i) * DM + 64 * g + 2 * c);
#pragma unroll
        for (int p = 0; p < 2; p++) {
          u32x4 h4;
#pragma unroll
          for (int i = 0; i < 4; i++) {
            const float v0 = __uint_as_float(p ? (cr[2 * i] & 0xffff0000u) : (cr[2 * i] << 16)) * NLOG2E;
            const float v1 = __uint_as_float(p ? (cr[2 * i + 1] & 0xffff0000u) : (cr[2 * i + 1] << 16)) * NLOG2E;
            h4[i] = cvt_pk_bf16(v0, v1);
          }
          *(u32x4*)(ws + WS_CF + (((g * 2 + p) * 2 + 0) * 64 + lane) * 16) = h4;
        }
      }
      return;
    }
    const u16* m0 = pair == 0 ? gw + 128 * DM : gw;
    const u16* m1 = pair == 0 ? vw : uw;
    u32 wr[2][8];
#pragma unroll
    for (int i = 0; i < 8; i++) {
      const int row = 16 * wave + 8 * hk + i;
      wr[0][i] = *(const u32*)(m0 + row * DM + 64 * g + 2 * c);
      wr[1][i] = *(const u32*)(m1 + row * DM + 64 * g + 2 * c);
    }
    u32x4 xv[7];
    const char* xb = (const char*)x + (size_t)(live ? b : 0) * (BN * 256);
#pragma unroll
    for (int q = 0; q < 7; q++) {
      const int ci = q * 64 + lane;
      xv[q] = *(const u32x4*)(xb + (u32)(ci < BN * 16 ? ci : 0) * 16u);
    }
    char* xt = lds + wave * (BN * XPITCH);
    char* zrow = lds + 8 * BN * XPITCH;
    if (tid < 17) *(u32x4*)(zrow + tid * 16) = u32x4{0, 0, 0, 0};
#pragma unroll
    for (int q = 0; q < 7; q++) {
      const int ci = q * 64 + lane;
      if (ci < BN * 16) *(u32x4*)(xt + (ci >> 4) * XPITCH + (ci & 15) * 16) = xv[q];
    }
    __syncthreads();
    bf16x8 af[8];
    {
      const char* rp = (c < BN ? xt + c * XPITCH : zrow) + hk * 16;
#pragma unroll
      for (int s = 0; s < 8; s++) af[s] = *(const bf16x8*)(rp + s * 32);
    }
    __syncthreads();
#pragma unroll
    for (int m = 0; m < 2; m++) {
      u32x4 fe, fo;
#pragma unroll
      for (int i = 0; i < 4; i++) {
        fe[i] = __builtin_amdgcn_perm(wr[m][2 * i + 1], wr[m][2 * i], 0x05040100u);
        fo[i] = __builtin_amdgcn_perm(wr[m][2 * i + 1], wr[m][2 * i], 0x07060302u);
      }
      *(u32x4*)(lds + ((wave * 4 + m * 2 + 0) * 64 + lane) * 16) = fe;
      *(u32x4*)(lds + ((wave * 4 + m * 2 + 1) * 64 + lane) * 16) = fo;
    }
    __syncthreads();
    f32x16 acc[4];
#pragma unroll
    for (int a = 0; a < 4; a++)
#pragma unroll
      for (int r = 0; r < 16; r++) acc[a][r] = 0.f;
#pragma unroll
    for (int s = 0; s < 8; s++) {
#pragma unroll
      for (int a = 0; a < 4; a++) {
        const bf16x8 bf = *(const bf16x8*)(lds + ((s * 4 + a) * 64 + lane) * 16);
        acc[a] = mfma32(af[s], bf, acc[a]);
      }
    }
    if (!live) return;
    if (pair == 0) {
      char* tt = lds + 32768 + wave * 4096;
      const u32 vo = (u32)c * 4u + (u32)hk * 512u;
#pragma unroll
      for (int p = 0; p < 2; p++) {
#pragma unroll
        for (int r = 0; r < 13; r++) {
          const u32 lo = cvt_pk_bf16(acc[2 + p][r], 0.f);
          const float xb = __builtin_amdgcn_fmed3f(__builtin_fmaf(acc[p][r], NLOG2E, 1e-30f), -3.3895314e38f, 3.3895314e38f);
          *(u32*)(tt + (vo + (u32)((r & 3) + 8 * (r >> 2)) * 128u)) =
              cvt_pk_bf16(acc[2 + p][r], __uint_as_float(__float_as_uint(xb) - lo));
        }
        const size_t tb = WS_T + (size_t)(2 * g + p) * T_BLK + (size_t)b * (BN * 128);
#pragma unroll
        for (int k = 0; k < 4; k++) {
          const u32x4 v = *(const u32x4*)(tt + k * 1024 + lane * 16);
          if (k < 3 || lane < 8) st16(ws, tb + k * 1024 + lane * 16, v);
        }
      }
      if (b == 0 && hk == 0) {
        *(u32*)(ws + WS_T + (size_t)(2 * g) * T_BLK + (size_t)N_NODES * 128 + c * 4) = 0u;
        *(u32*)(ws + WS_T + (size_t)(2 * g + 1) * T_BLK + (size_t)N_NODES * 128 + c * 4) = 0u;
      }
    } else {
#pragma unroll
      for (int p = 0; p < 2; p++)
#pragma unroll
        for (int s = 0; s < 2; s++) {
          u32x4 f;
#pragma unroll
          for (int i = 0; i < 4; i++)
            f[i] = cvt_pk_bf16(acc[p][8 * s + 2 * i] * NLOG2E, acc[p][8 * s + 2 * i + 1] * NLOG2E);
          st16(ws, WS_XA + (size_t)b * 8192 + (((2 * g + p) * 2 + s) * 64 + lane) * 16, f);
        }
#pragma unroll
      for (int q = 0; q < 4; q++) {
        u32x4 f;
#pragma unroll
        for (int i = 0; i < 4; i++) f[i] = cvt_pk_bf16(acc[2][4 * q + i], acc[3][4 * q + i]);
        st16(ws, WS_XU + (size_t)b * 8192 + ((g * 4 + q) * 64 + lane) * 16, f);
      }
    }
    return;
  }

  const int chunk = ridx;
  u32* cnt = (u32*)(lds + K1_STG);
  u32* img = (u32*)(lds + K1_IMG);
  u16* dpos = (u16*)(lds + K1_DPOS);
  u32* bst = (u32*)(lds + K1_BST);
  u32* wsum = (u32*)(lds + K1_WS);
  const bool act = tid < CH / 4;
  const int4 dv = ((const int4*)(dst + chunk * CH))[act ? tid : 0];
  const int4 sv = ((const int4*)(src + chunk * CH))[act ? tid : 0];
  u32x4 ev[7];
  const u32x4* ein = (const u32x4*)(eattr + (size_t)chunk * CH * DE);
#pragma unroll
  for (int k = 0; k < 7; k++) ev[k] = ein[(k < 6 || tid < 128) ? tid + 512 * k : 0];
  if (tid < 448) {
    *(u32x4*)(lds + tid * 32) = u32x4{0, 0, 0, 0};
    *(u32x4*)(lds + tid * 32 + 16) = u32x4{0, 0, 0, 0};
  }
  if (tid == 0) wsum[8] = 0u;
  __syncthreads();
  u32 bk[4], rk[4], rec[4];
#pragma unroll
  for (int i = 0; i < 4; i++) {
    const u32 d = (u32)(i == 0 ? dv.x : i == 1 ? dv.y : i == 2 ? dv.z : dv.w);
    const u32 sr = (u32)(i == 0 ? sv.x : i == 1 ? sv.y : i == 2 ? sv.z : sv.w);
    bk[i] = (d * 5243u) >> 17;
    rec[i] = (sr << 6) | rec_dbits(d - bk[i] * BN) | ((bk[i] & 1u) << 28) | ((bk[i] & 2u) << 26);
    rk[i] = act ? atomicAdd(&cnt[wave * 512 + bk[i]], 1u) : 0u;
  }
  __syncthreads();
  u32 tot = 0;
#pragma unroll
  for (int w = 0; w < 7; w++) {
    const u32 v = cnt[w * 512 + tid];
    cnt[w * 512 + tid] = tot;
    tot += v;
  }
  const u32 inc = wave_scan(tot);
  if (lane == 63) wsum[wave] = inc;
  __syncthreads();
  u32 wb = 0;
#pragma unroll
  for (int w = 0; w < 7; w++) wb += (w < wave) ? wsum[w] : 0u;
  bst[tid] = wb + inc - tot;
  __syncthreads();
  if (act) {
#pragma unroll
    for (int i = 0; i < 4; i++) {
      const u32 pos = bst[bk[i]] + cnt[wave * 512 + bk[i]] + rk[i];
      const u32 k = pos - bst[bk[i] & ~3u];
      dpos[tid * 4 + i] = (u16)pos;
      if (k < (u32)SCAP) {
        img[(bk[i] >> 2) * SLOT + k] = rec[i];
      } else {
        const u32 sp = atomicAdd(&wsum[8], 1u);
        ((u32x2*)(ws + WS_SPILL))[(size_t)chunk * CH + sp] = u32x2{bk[i] | ((u32)(chunk * CH) + pos) << 9, rec[i]};
      }
    }
  } else {
    const int sb = tid - CH / 4;
    const u32x4 q = *(const u32x4*)(bst + 4 * sb);
    const u32 e = bst[4 * sb + 4];
    *(u32x2*)(ws + WS_HT + (size_t)sb * 2048 + chunk * 8) = u32x2{min(q[1] - q[0], (u32)SCAP) | min(q[2] - q[0], (u32)SCAP) << 8 | min(q[3] - q[0], (u32)SCAP) << 16 |
                                       min(e - q[0], (u32)SCAP) << 24 | (e - q[0] > (u32)SCAP ? 0x80u : 0u),
                                       ((u32)(chunk * CH) + q[0]) * 32u};
  }
  if (tid < NSB - 112) {
    const int sb = 112 + tid;
    const u32x4 q = *(const u32x4*)(bst + 4 * sb);
    const u32 e = bst[4 * sb + 4];
    *(u32x2*)(ws + WS_HT + (size_t)sb * 2048 + chunk * 8) = u32x2{min(q[1] - q[0], (u32)SCAP) | min(q[2] - q[0], (u32)SCAP) << 8 | min(q[3] - q[0], (u32)SCAP) << 16 |
                                       min(e - q[0], (u32)SCAP) << 24 | (e - q[0] > (u32)SCAP ? 0x80u : 0u),
                                       ((u32)(chunk * CH) + q[0]) * 32u};
  }
  __syncthreads();
#pragma unroll
  for (int k = 0; k < 7; k++) {
    const int q = tid + 512 * k;
    if (k < 6 || tid < 128) *(u32x4*)(lds + K1_STG + (u32)dpos[q >> 1] * 32u + (q & 1) * 16) = ev[k];
  }
#pragma unroll
  for (int k = 0; k < 2; k++) {
    const int i = tid + 512 * k;
    if (i < NSB * 8)
      st16(ws, WS_SLOT + ((size_t)(i >> 3) * NCH + chunk) * (SLOT * 4) + (i & 7) * 16, *(const u32x4*)(lds + K1_IMG + i * 16));
  }
  if (tid == 0) ((u32*)(ws + WS_SPN))[chunk] = wsum[8];
  __syncthreads();
#pragma unroll
  for (int k = 0; k < 7; k++) {
    const int q = tid + 512 * k;
    if (k < 6 || tid < 128) st16(ws, WS_EC + (size_t)chunk * CH * 32 + q * 16, *(const u32x4*)(lds + K1_STG + q * 16));
  }
}

#define NWAVES 12
constexpr int NW = 3;
constexpr int NT = NW * 64;
constexpr int NTA = NSUB * NT;
constexpr int CAP = 960;
constexpr int CAPX = CAP + 32;
constexpr int IMG = 2048;
constexpr int G_IMG = 0;
constexpr int G_DUMP = G_IMG + NWAVES * 2 * IMG;
constexpr int G_RAW = G_DUMP + 64;
constexpr int G_SUB = G_RAW + NSUB * 8192;
constexpr int L_SO = 0;
constexpr int L_EO = L_SO + CAPX * 4;
constexpr int L_SA = L_EO + CAPX * 4;
constexpr int SUB_LDS = L_SA + CAPX * 4;
constexpr int G_TAB = G_SUB + NSUB * SUB_LDS;
constexpr int G_MISC = G_TAB + 4096;
constexpr int G_RED = G_MISC + 64;
constexpr int K2_LDS = G_RED + NWAVES * 2304;
static_assert(G_SUB % 16 == 0 && SUB_LDS % 16 == 0 && G_TAB % 16 == 0 && G_MISC % 16 == 0, "align");
static_assert(G_RAW < 65536 && CAPX * 8 <= 8192 && K2_LDS <= 163840, "layout");
constexpr u32 EO_OOB = 0x40000000u;
constexpr int RPT = CAP / NT;

#define EGC_LOOP_ASM \
  "s_setprio 3\n" \
  "v_mov_b32 v148, 0x3f80\n" \
  "ds_read_b32 v149, v145\n" \
  "ds_read_b128 v[128:131], v144 offset:0\n" \
  "ds_read_b128 v[132:135], v144 offset:16\n" \
  "ds_read_b128 v[136:139], v144 offset:32\n" \
  "ds_read_b128 v[140:143], v144 offset:48\n" \
  "ds_read_u16 v150, v146\n" \
  "s_mov_b32 exec_lo, 0\n" \
  "s_waitcnt lgkmcnt(5)\n" \
  "v_add_u32 v149, 16, v149\n" \
  "s_mov_b32 exec_lo, -1\n" \
  "buffer_load_dwordx4 v[112:115], v149, %[ers], 0 offen\n" \
  "s_waitcnt lgkmcnt(4)\n" \
  "buffer_load_dword v64, v128, %[trs], %[soff0] offen\n" \
  "buffer_load_dword v65, v129, %[trs], %[soff0] offen\n" \
  "buffer_load_dword v66, v130, %[trs], %[soff0] offen\n" \
  "buffer_load_dword v67, v131, %[trs], %[soff0] offen\n" \
  "s_waitcnt lgkmcnt(3)\n" \
  "buffer_load_dword v68, v132, %[trs], %[soff0] offen\n" \
  "buffer_load_dword v69, v133, %[trs], %[soff0] offen\n" \
  "buffer_load_dword v70, v134, %[trs], %[soff0] offen\n" \
  "buffer_load_dword v71, v135, %[trs], %[soff0] offen\n" \
  "s_waitcnt lgkmcnt(2)\n" \
  "buffer_load_dword v72, v136, %[trs], %[soff0] offen\n" \
  "buffer_load_dword v73, v137, %[trs], %[soff0] offen\n" \
  "buffer_load_dword v74, v138, %[trs], %[soff0] offen\n" \
  "buffer_load_dword v75, v139, %[trs], %[soff0] offen\n" \
  "s_waitcnt lgkmcnt(1)\n" \
  "buffer_load_dword v76, v140, %[trs], %[soff0] offen\n" \
  "buffer_load_dword v77, v141, %[trs], %[soff0] offen\n" \
  "buffer_load_dword v78, v142, %[trs], %[soff0] offen\n" \
  "buffer_load_dword v79, v143, %[trs], %[soff0] offen\n" \
  "s_waitcnt lgkmcnt(0)\n" \
  "ds_write_b16 v150, v148\n" \
  "ds_read_b128 v[120:123], v147 offset:2048\n" \
  "ds_read_b128 v[124:127], v157 offset:2048\n" \
  "LOOP_%=:\n" \
  "s_cmp_eq_u32 %[cnt], 5\n" \
  "s_cbranch_scc0 PA0_%=\n" \
  "s_setprio 2\n" \
  "PA0_%=:\n" \
  "s_cmp_eq_u32 %[cnt], 3\n" \
  "s_cbranch_scc0 PB0_%=\n" \
  "s_setprio 1\n" \
  "PB0_%=:\n" \
  "s_cmp_eq_u32 %[cnt], 1\n" \
  "s_cbranch_scc0 PC0_%=\n" \
  "s_setprio 0\n" \
  "PC0_%=:\n" \
  "s_cmp_eq_u32 %[cnt], 1\n" \
  "s_cbranch_scc0 PD0_%=\n" \
  "v_mbcnt_lo_u32_b32 v156, -1, 0\n" \
  "v_mbcnt_hi_u32_b32 v156, -1, v156\n" \
  "v_lshlrev_b32 v156, 4, v156\n" \
  "v_add_u32 v152, %[xuo], v156\n" \
  "v_and_b32 v156, 0x1f0, v156\n" \
  "global_load_dwordx4 v[152:155], v152, %[xup]\n" \
  "global_load_dword v156, v156, %[xup] offset:3072\n" \
  "PD0_%=:\n" \
  "s_waitcnt vmcnt(0)\n" \
  "v_mfma_f32_32x32x16_bf16 v[96:111], v[112:115], v[32:35], v[64:79]\n" \
  "buffer_load_dword v80, v128, %[trs], %[soff1] offen\n" \
  "buffer_load_dword v81, v129, %[trs], %[soff1] offen\n" \
  "buffer_load_dword v82, v130, %[trs], %[soff1] offen\n" \
  "buffer_load_dword v83, v131, %[trs], %[soff1] offen\n" \
  "buffer_load_dword v84, v132, %[trs], %[soff1] offen\n" \
  "buffer_load_dword v85, v133, %[trs], %[soff1] offen\n" \
  "s_waitcnt lgkmcnt(1)\n" \
  "v_mfma_f32_32x32x16_bf16 v[96:111], v[120:123], v[40:43], v[96:111]\n" \
  "buffer_load_dword v86, v134, %[trs], %[soff1] offen\n" \
  "buffer_load_dword v87, v135, %[trs], %[soff1] offen\n" \
  "buffer_load_dword v88, v136, %[trs], %[soff1] offen\n" \
  "buffer_load_dword v89, v137, %[trs], %[soff1] offen\n" \
  "buffer_load_dword v90, v138, %[trs], %[soff1] offen\n" \
  "buffer_load_dword v91, v139, %[trs], %[soff1] offen\n" \
  "s_waitcnt lgkmcnt(0)\n" \
  "v_mfma_f32_32x32x16_bf16 v[96:111], v[124:127], v[44:47], v[96:111]\n" \
  "buffer_load_dword v92, v140, %[trs], %[soff1] offen\n" \
  "buffer_load_dword v93, v141, %[trs], %[soff1] offen\n" \
  "buffer_load_dword v94, v142, %[trs], %[soff1] offen\n" \
  "buffer_load_dword v95, v143, %[trs], %[soff1] offen\n" \
  "s_add_u32 %[tq], %[tq], 3\n" \
  "s_cmp_lt_u32 %[tq], %[nt]\n" \
  "s_cselect_b32 %[tn], %[tq], 30\n" \
  "s_sub_u32 %[td], %[tn], %[tc]\n" \
  "s_lshl_b32 %[td], %[td], 7\n" \
  "s_mov_b32 %[tc], %[tn]\n" \
  "v_add_u32 v144, %[td], v144\n" \
  "v_add_u32 v145, %[td], v145\n" \
  "v_add_u32 v146, %[td], v146\n" \
  "ds_read_b32 v149, v145\n" \
  "ds_read_b128 v[128:131], v144 offset:0\n" \
  "ds_read_b128 v[132:135], v144 offset:16\n" \
  "ds_read_b128 v[136:139], v144 offset:32\n" \
  "ds_read_b128 v[140:143], v144 offset:48\n" \
  "ds_read_u16 v151, v146\n" \
  "ds_read_b128 v[36:39], v147\n" \
  "ds_read_b128 v[52:55], v157\n" \
  "ds_write_b16_d16_hi v150, v148\n" \
  "v_lshlrev_b32 v64, 16, v64\n" \
  "v_lshlrev_b32 v65, 16, v65\n" \
  "v_lshlrev_b32 v66, 16, v66\n" \
  "v_lshlrev_b32 v67, 16, v67\n" \
  "v_lshlrev_b32 v68, 16, v68\n" \
  "v_lshlrev_b32 v69, 16, v69\n" \
  "v_lshlrev_b32 v70, 16, v70\n" \
  "v_lshlrev_b32 v71, 16, v71\n" \
  "v_lshlrev_b32 v72, 16, v72\n" \
  "v_lshlrev_b32 v73, 16, v73\n" \
  "v_lshlrev_b32 v74, 16, v74\n" \
  "v_lshlrev_b32 v75, 16, v75\n" \
  "v_lshlrev_b32 v76, 16, v76\n" \
  "v_lshlrev_b32 v77, 16, v77\n" \
  "v_lshlrev_b32 v78, 16, v78\n" \
  "v_lshlrev_b32 v79, 16, v79\n" \
  "v_exp_f32 v96, v96\n" \
  "v_exp_f32 v97, v97\n" \
  "v_exp_f32 v98, v98\n" \
  "v_exp_f32 v99, v99\n" \
  "v_exp_f32 v100, v100\n" \
  "v_exp_f32 v101, v101\n" \
  "v_exp_f32 v102, v102\n" \
  "v_exp_f32 v103, v103\n" \
  "v_add_f32 v96, 1.0, v96\n" \
  "v_add_f32 v97, 1.0, v97\n" \
  "v_add_f32 v98, 1.0, v98\n" \
  "v_add_f32 v99, 1.0, v99\n" \
  "v_rcp_f32 v96, v96\n" \
  "v_rcp_f32 v97, v97\n" \
  "v_rcp_f32 v98, v98\n" \
  "v_rcp_f32 v99, v99\n" \
  "v_add_f32 v100, 1.0, v100\n" \
  "v_add_f32 v101, 1.0, v101\n" \
  "v_add_f32 v102, 1.0, v102\n" \
  "v_add_f32 v103, 1.0, v103\n" \
  "v_exp_f32 v104, v104\n" \
  "v_exp_f32 v105, v105\n" \
  "v_exp_f32 v106, v106\n" \
  "v_exp_f32 v107, v107\n" \
  "v_mul_f32 v64, v96, v64\n" \
  "v_mul_f32 v65, v97, v65\n" \
  "v_mul_f32 v66, v98, v66\n" \
  "v_mul_f32 v67, v99, v67\n" \
  "v_cvt_pk_bf16_f32 v96, v64, v65\n" \
  "v_cvt_pk_bf16_f32 v97, v66, v67\n" \
  "v_rcp_f32 v100, v100\n" \
  "v_rcp_f32 v101, v101\n" \
  "v_rcp_f32 v102, v102\n" \
  "v_rcp_f32 v103, v103\n" \
  "v_exp_f32 v108, v108\n" \
  "v_exp_f32 v109, v109\n" \
  "v_exp_f32 v110, v110\n" \
  "v_exp_f32 v111, v111\n" \
  "v_add_f32 v104, 1.0, v104\n" \
  "v_add_f32 v105, 1.0, v105\n" \
  "v_add_f32 v106, 1.0, v106\n" \
  "v_add_f32 v107, 1.0, v107\n" \
  "v_mul_f32 v68, v100, v68\n" \
  "v_mul_f32 v69, v101, v69\n" \
  "v_mul_f32 v70, v102, v70\n" \
  "v_mul_f32 v71, v103, v71\n" \
  "v_cvt_pk_bf16_f32 v98, v68, v69\n" \
  "v_cvt_pk_bf16_f32 v99, v70, v71\n" \
  "v_rcp_f32 v104, v104\n" \
  "v_rcp_f32 v105, v105\n" \
  "v_rcp_f32 v106, v106\n" \
  "v_rcp_f32 v107, v107\n" \
  "v_add_f32 v108, 1.0, v108\n" \
  "v_add_f32 v109, 1.0, v109\n" \
  "v_add_f32 v110, 1.0, v110\n" \
  "v_add_f32 v111, 1.0, v111\n" \
  "v_rcp_f32 v108, v108\n" \
  "v_rcp_f32 v109, v109\n" \
  "v_rcp_f32 v110, v110\n" \
  "v_rcp_f32 v111, v111\n" \
  "v_mul_f32 v72, v104, v72\n" \
  "v_mul_f32 v73, v105, v73\n" \
  "v_mul_f32 v74, v106, v74\n" \
  "v_mul_f32 v75, v107, v75\n" \
  "v_cvt_pk_bf16_f32 v100, v72, v73\n" \
  "v_cvt_pk_bf16_f32 v101, v74, v75\n" \
  "v_mul_f32 v76, v108, v76\n" \
  "v_mul_f32 v77, v109, v77\n" \
  "v_mul_f32 v78, v110, v78\n" \
  "v_mul_f32 v79, v111, v79\n" \
  "v_cvt_pk_bf16_f32 v102, v76, v77\n" \
  "v_cvt_pk_bf16_f32 v103, v78, v79\n" \
  "s_mov_b32 exec_lo, 0\n" \
  "s_waitcnt lgkmcnt(8)\n" \
  "v_add_u32 v149, 16, v149\n" \
  "s_mov_b32 exec_lo, -1\n" \
  "buffer_load_dwordx4 v[116:119], v149, %[ers], 0 offen\n" \
  "s_waitcnt lgkmcnt(2)\n" \
  "v_mfma_f32_32x32x16_bf16 v[0:15], v[36:39], v[96:99], v[0:15]\n" \
  "s_waitcnt lgkmcnt(1)\n" \
  "v_mfma_f32_32x32x16_bf16 v[0:15], v[52:55], v[100:103], v[0:15]\n" \
  "s_waitcnt vmcnt(1)\n" \
  "v_mfma_f32_32x32x16_bf16 v[96:111], v[112:115], v[48:51], v[80:95]\n" \
  "buffer_load_dword v64, v128, %[trs], %[soff0] offen\n" \
  "buffer_load_dword v65, v129, %[trs], %[soff0] offen\n" \
  "buffer_load_dword v66, v130, %[trs], %[soff0] offen\n" \
  "buffer_load_dword v67, v131, %[trs], %[soff0] offen\n" \
  "buffer_load_dword v68, v132, %[trs], %[soff0] offen\n" \
  "buffer_load_dword v69, v133, %[trs], %[soff0] offen\n" \
  "v_mfma_f32_32x32x16_bf16 v[96:111], v[120:123], v[56:59], v[96:111]\n" \
  "buffer_load_dword v70, v134, %[trs], %[soff0] offen\n" \
  "buffer_load_dword v71, v135, %[trs], %[soff0] offen\n" \
  "buffer_load_dword v72, v136, %[trs], %[soff0] offen\n" \
  "buffer_load_dword v73, v137, %[trs], %[soff0] offen\n" \
  "buffer_load_dword v74, v138, %[trs], %[soff0] offen\n" \
  "buffer_load_dword v75, v139, %[trs], %[soff0] offen\n" \
  "v_mfma_f32_32x32x16_bf16 v[96:111], v[124:127], v[60:63], v[96:111]\n" \
  "buffer_load_dword v76, v140, %[trs], %[soff0] offen\n" \
  "buffer_load_dword v77, v141, %[trs], %[soff0] offen\n" \
  "buffer_load_dword v78, v142, %[trs], %[soff0] offen\n" \
  "buffer_load_dword v79, v143, %[trs], %[soff0] offen\n" \
  "ds_write_b16 v151, v148\n" \
  "ds_read_b128 v[120:123], v147 offset:2048\n" \
  "ds_read_b128 v[124:127], v157 offset:2048\n" \
  "v_lshlrev_b32 v80, 16, v80\n" \
  "v_lshlrev_b32 v81, 16, v81\n" \
  "v_lshlrev_b32 v82, 16, v82\n" \
  "v_lshlrev_b32 v83, 16, v83\n" \
  "v_lshlrev_b32 v84, 16, v84\n" \
  "v_lshlrev_b32 v85, 16, v85\n" \
  "v_lshlrev_b32 v86, 16, v86\n" \
  "v_lshlrev_b32 v87, 16, v87\n" \
  "v_lshlrev_b32 v88, 16, v88\n" \
  "v_lshlrev_b32 v89, 16, v89\n" \
  "v_lshlrev_b32 v90, 16, v90\n" \
  "v_lshlrev_b32 v91, 16, v91\n" \
  "v_lshlrev_b32 v92, 16, v92\n" \
  "v_lshlrev_b32 v93, 16, v93\n" \
  "v_lshlrev_b32 v94, 16, v94\n" \
  "v_lshlrev_b32 v95, 16, v95\n" \
  "v_exp_f32 v96, v96\n" \
  "v_exp_f32 v97, v97\n" \
  "v_exp_f32 v98, v98\n" \
  "v_exp_f32 v99, v99\n" \
  "v_exp_f32 v100, v100\n" \
  "v_exp_f32 v101, v101\n" \
  "v_exp_f32 v102, v102\n" \
  "v_exp_f32 v103, v103\n" \
  "v_add_f32 v96, 1.0, v96\n" \
  "v_add_f32 v97, 1.0, v97\n" \
  "v_add_f32 v98, 1.0, v98\n" \
  "v_add_f32 v99, 1.0, v99\n" \
  "v_rcp_f32 v96, v96\n" \
  "v_rcp_f32 v97, v97\n" \
  "v_rcp_f32 v98, v98\n" \
  "v_rcp_f32 v99, v99\n" \
  "v_add_f32 v100, 1.0, v100\n" \
  "v_add_f32 v101, 1.0, v101\n" \
  "v_add_f32 v102, 1.0, v102\n" \
  "v_add_f32 v103, 1.0, v103\n" \
  "v_exp_f32 v104, v104\n" \
  "v_exp_f32 v105, v105\n" \
  "v_exp_f32 v106, v106\n" \
  "v_exp_f32 v107, v107\n" \
  "v_mul_f32 v80, v96, v80\n" \
  "v_mul_f32 v81, v97, v81\n" \
  "v_mul_f32 v82, v98, v82\n" \
  "v_mul_f32 v83, v99, v83\n" \
  "v_cvt_pk_bf16_f32 v96, v80, v81\n" \
  "v_cvt_pk_bf16_f32 v97, v82, v83\n" \
  "v_rcp_f32 v100, v100\n" \
  "v_rcp_f32 v101, v101\n" \
  "v_rcp_f32 v102, v102\n" \
  "v_rcp_f32 v103, v103\n" \
  "v_exp_f32 v108, v108\n" \
  "v_exp_f32 v109, v109\n" \
  "v_exp_f32 v110, v110\n" \
  "v_exp_f32 v111, v111\n" \
  "v_add_f32 v104, 1.0, v104\n" \
  "v_add_f32 v105, 1.0, v105\n" \
  "v_add_f32 v106, 1.0, v106\n" \
  "v_add_f32 v107, 1.0, v107\n" \
  "v_mul_f32 v84, v100, v84\n" \
  "v_mul_f32 v85, v101, v85\n" \
  "v_mul_f32 v86, v102, v86\n" \
  "v_mul_f32 v87, v103, v87\n" \
  "v_cvt_pk_bf16_f32 v98, v84, v85\n" \
  "v_cvt_pk_bf16_f32 v99, v86, v87\n" \
  "v_rcp_f32 v104, v104\n" \
  "v_rcp_f32 v105, v105\n" \
  "v_rcp_f32 v106, v106\n" \
  "v_rcp_f32 v107, v107\n" \
  "v_add_f32 v108, 1.0, v108\n" \
  "v_add_f32 v109, 1.0, v109\n" \
  "v_add_f32 v110, 1.0, v110\n" \
  "v_add_f32 v111, 1.0, v111\n" \
  "v_rcp_f32 v108, v108\n" \
  "v_rcp_f32 v109, v109\n" \
  "v_rcp_f32 v110, v110\n" \
  "v_rcp_f32 v111, v111\n" \
  "v_mul_f32 v88, v104, v88\n" \
  "v_mul_f32 v89, v105, v89\n" \
  "v_mul_f32 v90, v106, v90\n" \
  "v_mul_f32 v91, v107, v91\n" \
  "v_cvt_pk_bf16_f32 v100, v88, v89\n" \
  "v_cvt_pk_bf16_f32 v101, v90, v91\n" \
  "v_mul_f32 v92, v108, v92\n" \
  "v_mul_f32 v93, v109, v93\n" \
  "v_mul_f32 v94, v110, v94\n" \
  "v_mul_f32 v95, v111, v95\n" \
  "v_cvt_pk_bf16_f32 v102, v92, v93\n" \
  "v_cvt_pk_bf16_f32 v103, v94, v95\n" \
  "s_nop 1\n" \
  "v_mfma_f32_32x32x16_bf16 v[16:31], v[36:39], v[96:99], v[16:31]\n" \
  "v_mfma_f32_32x32x16_bf16 v[16:31], v[52:55], v[100:103], v[16:31]\n" \
  "s_sub_u32 %[cnt], %[cnt], 1\n" \
  "s_cmp_eq_u32 %[cnt], 0\n" \
  "s_cbranch_scc1 EXITA_%=\n" \
  "s_cmp_eq_u32 %[cnt], 5\n" \
  "s_cbranch_scc0 PA1_%=\n" \
  "s_setprio 2\n" \
  "PA1_%=:\n" \
  "s_cmp_eq_u32 %[cnt], 3\n" \
  "s_cbranch_scc0 PB1_%=\n" \
  "s_setprio 1\n" \
  "PB1_%=:\n" \
  "s_cmp_eq_u32 %[cnt], 1\n" \
  "s_cbranch_scc0 PC1_%=\n" \
  "s_setprio 0\n" \
  "PC1_%=:\n" \
  "s_cmp_eq_u32 %[cnt], 1\n" \
  "s_cbranch_scc0 PD1_%=\n" \
  "v_mbcnt_lo_u32_b32 v156, -1, 0\n" \
  "v_mbcnt_hi_u32_b32 v156, -1, v156\n" \
  "v_lshlrev_b32 v156, 4, v156\n" \
  "v_add_u32 v152, %[xuo], v156\n" \
  "v_and_b32 v156, 0x1f0, v156\n" \
  "global_load_dwordx4 v[152:155], v152, %[xup]\n" \
  "global_load_dword v156, v156, %[xup] offset:3072\n" \
  "PD1_%=:\n" \
  "s_waitcnt vmcnt(0)\n" \
  "v_mfma_f32_32x32x16_bf16 v[96:111], v[116:119], v[32:35], v[64:79]\n" \
  "buffer_load_dword v80, v128, %[trs], %[soff1] offen\n" \
  "buffer_load_dword v81, v129, %[trs], %[soff1] offen\n" \
  "buffer_load_dword v82, v130, %[trs], %[soff1] offen\n" \
  "buffer_load_dword v83, v131, %[trs], %[soff1] offen\n" \
  "buffer_load_dword v84, v132, %[trs], %[soff1] offen\n" \
  "buffer_load_dword v85, v133, %[trs], %[soff1] offen\n" \
  "s_waitcnt lgkmcnt(1)\n" \
  "v_mfma_f32_32x32x16_bf16 v[96:111], v[120:123], v[40:43], v[96:111]\n" \
  "buffer_load_dword v86, v134, %[trs], %[soff1] offen\n" \
  "buffer_load_dword v87, v135, %[trs], %[soff1] offen\n" \
  "buffer_load_dword v88, v136, %[trs], %[soff1] offen\n" \
  "buffer_load_dword v89, v137, %[trs], %[soff1] offen\n" \
  "buffer_load_dword v90, v138, %[trs], %[soff1] offen\n" \
  "buffer_load_dword v91, v139, %[trs], %[soff1] offen\n" \
  "s_waitcnt lgkmcnt(0)\n" \
  "v_mfma_f32_32x32x16_bf16 v[96:111], v[124:127], v[44:47], v[96:111]\n" \
  "buffer_load_dword v92, v140, %[trs], %[soff1] offen\n" \
  "buffer_load_dword v93, v141, %[trs], %[soff1] offen\n" \
  "buffer_load_dword v94, v142, %[trs], %[soff1] offen\n" \
  "buffer_load_dword v95, v143, %[trs], %[soff1] offen\n" \
  "s_add_u32 %[tq], %[tq], 3\n" \
  "s_cmp_lt_u32 %[tq], %[nt]\n" \
  "s_cselect_b32 %[tn], %[tq], 30\n" \
  "s_sub_u32 %[td], %[tn], %[tc]\n" \
  "s_lshl_b32 %[td], %[td], 7\n" \
  "s_mov_b32 %[tc], %[tn]\n" \
  "v_add_u32 v144, %[td], v144\n" \
  "v_add_u32 v145, %[td], v145\n" \
  "v_add_u32 v146, %[td], v146\n" \
  "ds_read_b32 v149, v145\n" \
  "ds_read_b128 v[128:131], v144 offset:0\n" \
  "ds_read_b128 v[132:135], v144 offset:16\n" \
  "ds_read_b128 v[136:139], v144 offset:32\n" \
  "ds_read_b128 v[140:143], v144 offset:48\n" \
  "ds_read_u16 v150, v146\n" \
  "ds_read_b128 v[36:39], v147\n" \
  "ds_read_b128 v[52:55], v157\n" \
  "ds_write_b16_d16_hi v151, v148\n" \
  "v_lshlrev_b32 v64, 16, v64\n" \
  "v_lshlrev_b32 v65, 16, v65\n" \
  "v_lshlrev_b32 v66, 16, v66\n" \
  "v_lshlrev_b32 v67, 16, v67\n" \
  "v_lshlrev_b32 v68, 16, v68\n" \
  "v_lshlrev_b32 v69, 16, v69\n" \
  "v_lshlrev_b32 v70, 16, v70\n" \
  "v_lshlrev_b32 v71, 16, v71\n" \
  "v_lshlrev_b32 v72, 16, v72\n" \
  "v_lshlrev_b32 v73, 16, v73\n" \
  "v_lshlrev_b32 v74, 16, v74\n" \
  "v_lshlrev_b32 v75, 16, v75\n" \
  "v_lshlrev_b32 v76, 16, v76\n" \
  "v_lshlrev_b32 v77, 16, v77\n" \
  "v_lshlrev_b32 v78, 16, v78\n" \
  "v_lshlrev_b32 v79, 16, v79\n" \
  "v_exp_f32 v96, v96\n" \
  "v_exp_f32 v97, v97\n" \
  "v_exp_f32 v98, v98\n" \
  "v_exp_f32 v99, v99\n" \
  "v_exp_f32 v100, v100\n" \
  "v_exp_f32 v101, v101\n" \
  "v_exp_f32 v102, v102\n" \
  "v_exp_f32 v103, v103\n" \
  "v_add_f32 v96, 1.0, v96\n" \
  "v_add_f32 v97, 1.0, v97\n" \
  "v_add_f32 v98, 1.0, v98\n" \
  "v_add_f32 v99, 1.0, v99\n" \
  "v_rcp_f32 v96, v96\n" \
  "v_rcp_f32 v97, v97\n" \
  "v_rcp_f32 v98, v98\n" \
  "v_rcp_f32 v99, v99\n" \
  "v_add_f32 v100, 1.0, v100\n" \
  "v_add_f32 v101, 1.0, v101\n" \
  "v_add_f32 v102, 1.0, v102\n" \
  "v_add_f32 v103, 1.0, v103\n" \
  "v_exp_f32 v104, v104\n" \
  "v_exp_f32 v105, v105\n" \
  "v_exp_f32 v106, v106\n" \
  "v_exp_f32 v107, v107\n" \
  "v_mul_f32 v64, v96, v64\n" \
  "v_mul_f32 v65, v97, v65\n" \
  "v_mul_f32 v66, v98, v66\n" \
  "v_mul_f32 v67, v99, v67\n" \
  "v_cvt_pk_bf16_f32 v96, v64, v65\n" \
  "v_cvt_pk_bf16_f32 v97, v66, v67\n" \
  "v_rcp_f32 v100, v100\n" \
  "v_rcp_f32 v101, v101\n" \
  "v_rcp_f32 v102, v102\n" \
  "v_rcp_f32 v103, v103\n" \
  "v_exp_f32 v108, v108\n" \
  "v_exp_f32 v109, v109\n" \
  "v_exp_f32 v110, v110\n" \
  "v_exp_f32 v111, v111\n" \
  "v_add_f32 v104, 1.0, v104\n" \
  "v_add_f32 v105, 1.0, v105\n" \
  "v_add_f32 v106, 1.0, v106\n" \
  "v_add_f32 v107, 1.0, v107\n" \
  "v_mul_f32 v68, v100, v68\n" \
  "v_mul_f32 v69, v101, v69\n" \
  "v_mul_f32 v70, v102, v70\n" \
  "v_mul_f32 v71, v103, v71\n" \
  "v_cvt_pk_bf16_f32 v98, v68, v69\n" \
  "v_cvt_pk_bf16_f32 v99, v70, v71\n" \
  "v_rcp_f32 v104, v104\n" \
  "v_rcp_f32 v105, v105\n" \
  "v_rcp_f32 v106, v106\n" \
  "v_rcp_f32 v107, v107\n" \
  "v_add_f32 v108, 1.0, v108\n" \
  "v_add_f32 v109, 1.0, v109\n" \
  "v_add_f32 v110, 1.0, v110\n" \
  "v_add_f32 v111, 1.0, v111\n" \
  "v_rcp_f32 v108, v108\n" \
  "v_rcp_f32 v109, v109\n" \
  "v_rcp_f32 v110, v110\n" \
  "v_rcp_f32 v111, v111\n" \
  "v_mul_f32 v72, v104, v72\n" \
  "v_mul_f32 v73, v105, v73\n" \
  "v_mul_f32 v74, v106, v74\n" \
  "v_mul_f32 v75, v107, v75\n" \
  "v_cvt_pk_bf16_f32 v100, v72, v73\n" \
  "v_cvt_pk_bf16_f32 v101, v74, v75\n" \
  "v_mul_f32 v76, v108, v76\n" \
  "v_mul_f32 v77, v109, v77\n" \
  "v_mul_f32 v78, v110, v78\n" \
  "v_mul_f32 v79, v111, v79\n" \
  "v_cvt_pk_bf16_f32 v102, v76, v77\n" \
  "v_cvt_pk_bf16_f32 v103, v78, v79\n" \
  "s_mov_b32 exec_lo, 0\n" \
  "s_waitcnt lgkmcnt(8)\n" \
  "v_add_u32 v149, 16, v149\n" \
  "s_mov_b32 exec_lo, -1\n" \
  "buffer_load_dwordx4 v[112:115], v149, %[ers], 0 offen\n" \
  "s_waitcnt lgkmcnt(2)\n" \
  "v_mfma_f32_32x32x16_bf16 v[0:15], v[36:39], v[96:99], v[0:15]\n" \
  "s_waitcnt lgkmcnt(1)\n" \
  "v_mfma_f32_32x32x16_bf16 v[0:15], v[52:55], v[100:103], v[0:15]\n" \
  "s_waitcnt vmcnt(1)\n" \
  "v_mfma_f32_32x32x16_bf16 v[96:111], v[116:119], v[48:51], v[80:95]\n" \
  "buffer_load_dword v64, v128, %[trs], %[soff0] offen\n" \
  "buffer_load_dword v65, v129, %[trs], %[soff0] offen\n" \
  "buffer_load_dword v66, v130, %[trs], %[soff0] offen\n" \
  "buffer_load_dword v67, v131, %[trs], %[soff0] offen\n" \
  "buffer_load_dword v68, v132, %[trs], %[soff0] offen\n" \
  "buffer_load_dword v69, v133, %[trs], %[soff0] offen\n" \
  "v_mfma_f32_32x32x16_bf16 v[96:111], v[120:123], v[56:59], v[96:111]\n" \
  "buffer_load_dword v70, v134, %[trs], %[soff0] offen\n" \
  "buffer_load_dword v71, v135, %[trs], %[soff0] offen\n" \
  "buffer_load_dword v72, v136, %[trs], %[soff0] offen\n" \
  "buffer_load_dword v73, v137, %[trs], %[soff0] offen\n" \
  "buffer_load_dword v74, v138, %[trs], %[soff0] offen\n" \
  "buffer_load_dword v75, v139, %[trs], %[soff0] offen\n" \
  "v_mfma_f32_32x32x16_bf16 v[96:111], v[124:127], v[60:63], v[96:111]\n" \
  "buffer_load_dword v76, v140, %[trs], %[soff0] offen\n" \
  "buffer_load_dword v77, v141, %[trs], %[soff0] offen\n" \
  "buffer_load_dword v78, v142, %[trs], %[soff0] offen\n" \
  "buffer_load_dword v79, v143, %[trs], %[soff0] offen\n" \
  "ds_write_b16 v150, v148\n" \
  "ds_read_b128 v[120:123], v147 offset:2048\n" \
  "ds_read_b128 v[124:127], v157 offset:2048\n" \
  "v_lshlrev_b32 v80, 16, v80\n" \
  "v_lshlrev_b32 v81, 16, v81\n" \
  "v_lshlrev_b32 v82, 16, v82\n" \
  "v_lshlrev_b32 v83, 16, v83\n" \
  "v_lshlrev_b32 v84, 16, v84\n" \
  "v_lshlrev_b32 v85, 16, v85\n" \
  "v_lshlrev_b32 v86, 16, v86\n" \
  "v_lshlrev_b32 v87, 16, v87\n" \
  "v_lshlrev_b32 v88, 16, v88\n" \
  "v_lshlrev_b32 v89, 16, v89\n" \
  "v_lshlrev_b32 v90, 16, v90\n" \
  "v_lshlrev_b32 v91, 16, v91\n" \
  "v_lshlrev_b32 v92, 16, v92\n" \
  "v_lshlrev_b32 v93, 16, v93\n" \
  "v_lshlrev_b32 v94, 16, v94\n" \
  "v_lshlrev_b32 v95, 16, v95\n" \
  "v_exp_f32 v96, v96\n" \
  "v_exp_f32 v97, v97\n" \
  "v_exp_f32 v98, v98\n" \
  "v_exp_f32 v99, v99\n" \
  "v_exp_f32 v100, v100\n" \
  "v_exp_f32 v101, v101\n" \
  "v_exp_f32 v102, v102\n" \
  "v_exp_f32 v103, v103\n" \
  "v_add_f32 v96, 1.0, v96\n" \
  "v_add_f32 v97, 1.0, v97\n" \
  "v_add_f32 v98, 1.0, v98\n" \
  "v_add_f32 v99, 1.0, v99\n" \
  "v_rcp_f32 v96, v96\n" \
  "v_rcp_f32 v97, v97\n" \
  "v_rcp_f32 v98, v98\n" \
  "v_rcp_f32 v99, v99\n" \
  "v_add_f32 v100, 1.0, v100\n" \
  "v_add_f32 v101, 1.0, v101\n" \
  "v_add_f32 v102, 1.0, v102\n" \
  "v_add_f32 v103, 1.0, v103\n" \
  "v_exp_f32 v104, v104\n" \
  "v_exp_f32 v105, v105\n" \
  "v_exp_f32 v106, v106\n" \
  "v_exp_f32 v107, v107\n" \
  "v_mul_f32 v80, v96, v80\n" \
  "v_mul_f32 v81, v97, v81\n" \
  "v_mul_f32 v82, v98, v82\n" \
  "v_mul_f32 v83, v99, v83\n" \
  "v_cvt_pk_bf16_f32 v96, v80, v81\n" \
  "v_cvt_pk_bf16_f32 v97, v82, v83\n" \
  "v_rcp_f32 v100, v100\n" \
  "v_rcp_f32 v101, v101\n" \
  "v_rcp_f32 v102, v102\n" \
  "v_rcp_f32 v103, v103\n" \
  "v_exp_f32 v108, v108\n" \
  "v_exp_f32 v109, v109\n" \
  "v_exp_f32 v110, v110\n" \
  "v_exp_f32 v111, v111\n" \
  "v_add_f32 v104, 1.0, v104\n" \
  "v_add_f32 v105, 1.0, v105\n" \
  "v_add_f32 v106, 1.0, v106\n" \
  "v_add_f32 v107, 1.0, v107\n" \
  "v_mul_f32 v84, v100, v84\n" \
  "v_mul_f32 v85, v101, v85\n" \
  "v_mul_f32 v86, v102, v86\n" \
  "v_mul_f32 v87, v103, v87\n" \
  "v_cvt_pk_bf16_f32 v98, v84, v85\n" \
  "v_cvt_pk_bf16_f32 v99, v86, v87\n" \
  "v_rcp_f32 v104, v104\n" \
  "v_rcp_f32 v105, v105\n" \
  "v_rcp_f32 v106, v106\n" \
  "v_rcp_f32 v107, v107\n" \
  "v_add_f32 v108, 1.0, v108\n" \
  "v_add_f32 v109, 1.0, v109\n" \
  "v_add_f32 v110, 1.0, v110\n" \
  "v_add_f32 v111, 1.0, v111\n" \
  "v_rcp_f32 v108, v108\n" \
  "v_rcp_f32 v109, v109\n" \
  "v_rcp_f32 v110, v110\n" \
  "v_rcp_f32 v111, v111\n" \
  "v_mul_f32 v88, v104, v88\n" \
  "v_mul_f32 v89, v105, v89\n" \
  "v_mul_f32 v90, v106, v90\n" \
  "v_mul_f32 v91, v107, v91\n" \
  "v_cvt_pk_bf16_f32 v100, v88, v89\n" \
  "v_cvt_pk_bf16_f32 v101, v90, v91\n" \
  "v_mul_f32 v92, v108, v92\n" \
  "v_mul_f32 v93, v109, v93\n" \
  "v_mul_f32 v94, v110, v94\n" \
  "v_mul_f32 v95, v111, v95\n" \
  "v_cvt_pk_bf16_f32 v102, v92, v93\n" \
  "v_cvt_pk_bf16_f32 v103, v94, v95\n" \
  "s_nop 1\n" \
  "v_mfma_f32_32x32x16_bf16 v[16:31], v[36:39], v[96:99], v[16:31]\n" \
  "v_mfma_f32_32x32x16_bf16 v[16:31], v[52:55], v[100:103], v[16:31]\n" \
  "s_sub_u32 %[cnt], %[cnt], 1\n" \
  "s_cmp_eq_u32 %[cnt], 0\n" \
  "s_cbranch_scc0 LOOP_%=\n" \
  "ds_write_b16_d16_hi v150, v148\n" \
  "s_branch DONE_%=\n" \
  "EXITA_%=:\n" \
  "ds_write_b16_d16_hi v151, v148\n" \
  "DONE_%=:\n" \
  "s_waitcnt vmcnt(0) lgkmcnt(0)\n" \
  "s_nop 15\n"
#define EGC_LOOP_CLOBBERS "v64", "v65", "v66", "v67", "v68", "v69", "v70", "v71", "v72", "v73", "v74", "v75", "v76", "v77", "v78", "v79", "v80", "v81", "v82", "v83", "v84", "v85", "v86", "v87", "v88", "v89", "v90", "v91", "v92", "v93", "v94", "v95", "v96", "v97", "v98", "v99", "v100", "v101", "v102", "v103", "v104", "v105", "v106", "v107", "v108", "v109", "v110", "v111", "v112", "v113", "v114", "v115", "v116", "v117", "v118", "v119", "v120", "v121", "v122", "v123", "v124", "v125", "v126", "v127", "v128", "v129", "v130", "v131", "v132", "v133", "v134", "v135", "v136", "v137", "v138", "v139", "v140", "v141", "v142", "v143", "v148", "v149", "v150", "v151", "v36", "v37", "v38", "v39", "v52", "v53", "v54", "v55"

__global__ __launch_bounds__(NTA, NW) void egc_edge(const u16* __restrict__ gw, const char* __restrict__ ws,
                                                    u16* __restrict__ out) {
  __shared__ __attribute__((aligned(4096))) char lds_all[K2_LDS];
  const int wv = __builtin_amdgcn_readfirstlane(threadIdx.x >> 6);
  const int sub = wv / NW, wave = wv - sub * NW;
  const int g = blockIdx.x & 1, sb = blockIdx.x >> 1;
  const int b = sb * NSUB + sub;
  char* lds = lds_all + G_SUB + sub * SUB_LDS;
  const u32 l0 = (u32)(unsigned long long)(__attribute__((address_space(3))) char*)lds_all;
  const u32 lb = l0 + G_SUB + sub * SUB_LDS;
  const u32 li = l0 + G_IMG + sub * (NW * 2 * IMG);
  u32* so = (u32*)(lds + L_SO);
  u32* eo = (u32*)(lds + L_EO);
  u32* sa = (u32*)(lds + L_SA);
  u32* misc = (u32*)(lds_all + G_MISC);

  bf16x8 xaf[2][2], chi[2];
  f32x16 acc[2];
  const char* slots = ws + WS_SLOT + (size_t)sb * (NCH * SLOT * 4);
  const unsigned long long tp = (unsigned long long)(ws + WS_T - 128), ep = (unsigned long long)(ws + WS_EC);
  const u32x4 trs = {(u32)tp, ((u32)(tp >> 32) & 0xffffu) | (4u << 16), 0x7fffffffu, 1u << 23};
  const u32x4 ers = {(u32)ep, (u32)(ep >> 32) & 0xffffu, (u32)(N_EDGES * 32), 0x00020000u};
  const int soff0 = (int)((2 * g) * T_BLK), soff1 = (int)((2 * g + 1) * T_BLK);

  u32x2* raw = (u32x2*)(lds_all + G_RAW + sub * 8192);
  u32x4 xu;
  u32 xu24;
  const char* xup = ws + WS_XU + (size_t)b * 8192 + g * 4096;
  const char* ht = ws + WS_HT + (size_t)sb * 2048;
  auto slot_round = [&](const int base, const bool first, int& mx, bool& spill) __attribute__((always_inline)) -> int {
    const u32 lane = (u32)fresh_lane();
    const u32 ta = (u32)wv * 64u + lane;
    const u32x4 h0 = *(const u32x4*)(ht + lane * 16u), h1 = *(const u32x4*)(ht + (1024u + lane * 16u));
    u32x4 rq[3];
#pragma unroll
    for (int k = 0; k < 3; k++) {
      const u32 idx = ta + k * NTA;
      rq[k] = *(const u32x4*)(slots + ((k < 2 || idx < NCH * 8) ? idx * 16u : 0u));
    }
    if (first) {
#pragma unroll
      for (int i = 0; i < 2 * IMG / 1024; i++) *(u32x4*)(lds_all + G_IMG + wv * (2 * IMG) + i * 1024 + lane * 16) = u32x4{0, 0, 0, 0};
    }
    const bool in1 = lane < (NCH - 128) / 2;
    const u32 hf[4] = {h0[0], h0[2], in1 ? h1[0] : 0u, in1 ? h1[2] : 0u}, eb[4] = {h0[1], h0[3], h1[1], h1[3]};
    u32 na[4], nb[4], la = 0, lb_ = 0;
#pragma unroll
    for (int i = 0; i < 4; i++) {
      const u32 h = hf[i] & ~0x80u, n = h - (h << 8);
      na[i] = n & 0x00ff00ffu;
      nb[i] = (n >> 8) & 0x00ff00ffu;
      la += na[i];
      lb_ += nb[i];
    }
    const u32 sa_ = wave_scan(la), sb_ = wave_scan(lb_);
    const u32 ta_ = (u32)__builtin_amdgcn_readlane((int)sa_, 63), tb_ = (u32)__builtin_amdgcn_readlane((int)sb_, 63);
    const u32 tv = (sub & 1) ? tb_ : ta_;
    const int M = (int)((sub & 2) ? tv >> 16 : tv & 0xffffu);
    mx = (int)max(max(ta_ & 0xffffu, ta_ >> 16), max(tb_ & 0xffffu, tb_ >> 16));
    int m = M - base;
    m = m < 0 ? 0 : (m > CAP ? CAP : m);
    const bool fast = first && mx <= CAP;
    {
      u32 pa = sa_ - la + 0x00200020u, pb = sb_ - lb_ + 0x00200020u;
#pragma unroll
      for (int i = 0; i < 4; i++) {
        const u32 h = hf[i] & ~0x80u;
        const u32 qa = pa - ((h << 8) & 0x00ff00ffu), qb = pb - (h & 0x00ff00ffu);
        *(u32x4*)(lds_all + G_TAB + ((i >> 1) * 2048u + (i & 1) * 16u + lane * 32u)) =
            u32x4{fast ? (qa << 3) + 0x40000000u : qa, fast ? (qb << 3) + 0x60002000u : qb, h >> 24, eb[i]};
        pa += na[i];
        pb += nb[i];
      }
    }
    const unsigned long long spm = __ballot(((hf[0] | hf[1] | hf[2] | hf[3]) & 0x80u) != 0u);
    spill = spm != 0ull;
    if (first) {
      asm volatile("" ::: "memory");
      const char* cf = ws + WS_CF + g * 4096;
      const char* xa = ws + WS_XA + (size_t)b * 8192 + g * 4096;
#pragma unroll
      for (int p = 0; p < 2; p++) {
        xaf[p][0] = *(const bf16x8*)(xa + (lane * 16u + p * 2048u));
        xaf[p][1] = *(const bf16x8*)(xa + (lane * 16u + p * 2048u + 1024u));
        chi[p] = *(const bf16x8*)(cf + (lane * 16u + p * 2048u));
      }
    }
    const u32 q = lane & 7u;
    u32x4 te[3];
    u32 to[3];
#pragma unroll
    for (int k = 0; k < 3; k++) {
      to[k] = G_TAB + min((ta + k * NTA) >> 3, 255u) * 16u;
      te[k] = *(const u32x4*)(lds_all + to[k]);
    }
    if (fast) {
      const u32 c8 = q * 32u + (l0 + G_RAW - 256u);
#pragma unroll
      for (int k = 0; k < 3; k++) {
        const int n = (int)te[k][2] - (int)(4u * q);
        const u32 e0 = te[k][3] + q * 128u;
        u32 a0, a1, a2, a3, e1, e2, e3;
        asm volatile(
            "v_lshrrev_b32 %0, 26, %7\n\tv_lshrrev_b32 %1, 26, %8\n\tv_lshrrev_b32 %2, 26, %9\n\tv_lshrrev_b32 %3, 26, %10\n\t"
            "v_and_or_b32 %0, %0, 6, %11\n\tv_and_or_b32 %1, %1, 6, %11\n\t"
            "v_and_or_b32 %2, %2, 6, %11\n\tv_and_or_b32 %3, %3, 6, %11\n\t"
            "ds_read_u16 %0, %0\n\tds_read_u16 %1, %1\n\tds_read_u16 %2, %2\n\tds_read_u16 %3, %3\n\t"
            "v_add_u32 %4, 32, %14\n\tv_add_u32 %5, 64, %14\n\tv_add_u32 %6, 0x60, %14\n\t"
            "s_waitcnt lgkmcnt(0)\n\t"
            "v_add_u32 %0, %0, %12\n\tv_add_u32 %1, %1, %12\n\tv_add_u32 %2, %2, %12\n\tv_add_u32 %3, %3, %12\n\t"
            "v_cmpx_lt_i32 vcc, 0, %13\n\tds_write2_b32 %0, %7, %14 offset0:0 offset1:1\n\t"
            "v_cmpx_lt_i32 vcc, 1, %13\n\tds_write2_b32 %1, %8, %4 offset0:2 offset1:3\n\t"
            "v_cmpx_lt_i32 vcc, 2, %13\n\tds_write2_b32 %2, %9, %5 offset0:4 offset1:5\n\t"
            "v_cmpx_lt_i32 vcc, 3, %13\n\tds_write2_b32 %3, %10, %6 offset0:6 offset1:7\n\t"
            "s_mov_b64 exec, -1"
            : "=&v"(a0), "=&v"(a1), "=&v"(a2), "=&v"(a3), "=&v"(e1), "=&v"(e2), "=&v"(e3)
            : "v"(rq[k][0]), "v"(rq[k][1]), "v"(rq[k][2]), "v"(rq[k][3]), "v"(l0 + to[k]), "v"(c8), "v"(n), "v"(e0)
            : "memory", "vcc");
      }
    } else {
#pragma unroll
      for (int k = 0; k < 3; k++) {
        const int n = (int)te[k][2] - (int)(4u * q);
#pragma unroll
        for (int j = 0; j < 4; j++) {
          const u32 r = rq[k][j];
          const u32 sbk = ((r >> 28) & 1u) | ((r >> 26) & 2u);
          const u32 pw = (sbk & 1u) ? te[k][1] : te[k][0];
          const u32 o = ((sbk & 2u) ? pw >> 16 : pw & 0xffffu) - 32u + 4u * q + j - (u32)base;
          if (j < n && o < (u32)CAP)
            *(u32x2*)(lds_all + G_RAW + sbk * 8192u + o * 8u) = u32x2{r, te[k][3] + (4u * q + j) * 32u};
        }
      }
    }
    if (fast && spill && (spm & (spm - 1)) == 0ull) {
      const int fl = __builtin_ctzll(spm);
      u32 info = 0;
#pragma unroll
      for (int i = 0; i < 4; i++)
        if ((u32)__builtin_amdgcn_readlane((int)hf[i], fl) & 0x80u)
          info = ((info >> 16) + 1u) << 16 | (u32)((i >> 1) * 128 + 2 * fl + (i & 1));
      const u32 ch = info & 0xffffu;
      const int n = (int)((const u32*)(ws + WS_SPN))[ch];
      if ((info >> 16) == 1u && n <= 64 && mx + n <= CAP) {
        u32x2 v = {0xffffffffu, 0u};
        if ((int)lane < n) v = ((const u32x2*)(ws + WS_SPILL))[(size_t)ch * CH + lane];
        const bool ok = (int)lane < n && (v[0] & 511u) == (u32)b;
        const unsigned long long mk = __ballot(ok);
        if (ok && wave == 0)
          raw[m + (int)__builtin_amdgcn_mbcnt_hi((u32)(mk >> 32), __builtin_amdgcn_mbcnt_lo((u32)mk, 0u))] = u32x2{v[1], (v[0] >> 9) * 32u};
        m += __popcll(mk);
        spill = false;
      }
    }
    return m;
  };

  auto run_list = [&](const int m) __attribute__((always_inline)) {
    const int lane = fresh_lane();
    const int tid = wave * 64 + lane, c = lane & 31, hk = lane >> 5;
    const int nt = (m + 31) >> 5;
    if (tid < 32) raw[m + tid] = u32x2{REC_PAD, EO_OOB};
    __syncthreads();
    {
      const u32 h = ((u32)c >> 4) & 1u, i = (u32)c & 15u;
      const u32 mrow = (i & 3u) + 8u * (i >> 2) + 4u * h;
      const u32 ib = li + wave * (2 * IMG);
      const u32 kc = (ib + IMG + mrow * 64u + ((mrow >> 2) & 3u) * 16u) | (ib + (i >> 3) * 32u + h * 16u + (i & 7u) * 2u) << 16;
      const u32 hb = 128u - h * 128u;
      const int p0 = (wave + NW * hk) * 32 + c;
      u32x2 rr[RPT];
#pragma unroll
      for (int k = 0; k < RPT; k++) rr[k] = raw[p0 + k * NT];
#pragma unroll
      for (int k = 0; k < RPT; k++) {
        const int p = p0 + k * NT;
        so[p] = ((rr[k][0] & 0xfffc0u) << 1) + hb;
        sa[p] = (rr[k][0] & 0x07f0003eu) ^ kc;
        eo[p] = rr[k][1];
      }
      if (hk == 0) {
        so[CAP + c] = (u32)N_NODES * 128u + hb;
        sa[CAP + c] = (l0 + G_DUMP) * 0x10001u;
        eo[CAP + c] = EO_OOB;
      }
    }
    int cntw = (nt - wave + NW - 1) / NW;
    if (cntw > 0) {
      const int pm = ((c >> 2) & 1) * 16 + (c & 3) + 4 * (c >> 3);
      u32 a_so = lb + L_SO + wave * 128 + hk * 64, a_eo = lb + L_EO + wave * 128 + pm * 4;
      u32 a_sa = lb + L_SA + wave * 128 + c * 4 + hk * 2;
      const u32 a_img = li + wave * 2 * IMG + c * 64 + ((hk ^ ((c >> 2) & 3)) << 4);
      int tq = wave, tc = wave, tn, td;
      asm volatile(EGC_LOOP_ASM
                   : "+{v[0:15]}"(acc[0]), "+{v[16:31]}"(acc[1]), "+{v144}"(a_so), "+{v145}"(a_eo), "+{v146}"(a_sa),
                     "={v[152:155]}"(xu), "={v156}"(xu24),
                     [cnt] "+s"(cntw), [tq] "+s"(tq), [tc] "+s"(tc), [tn] "=&s"(tn), [td] "=&s"(td)
                   : "{v[32:35]}"(chi[0]), "{v[40:43]}"(xaf[0][0]), "{v[44:47]}"(xaf[0][1]),
                     "{v[48:51]}"(chi[1]), "{v[56:59]}"(xaf[1][0]), "{v[60:63]}"(xaf[1][1]),
                     "{v147}"(a_img), "{v157}"(a_img ^ 32u),
                     [trs] "s"(trs), [ers] "s"(ers), [soff0] "s"(soff0), [soff1] "s"(soff1), [nt] "s"(nt),
                     [xup] "s"(xup), [xuo] "s"(wave * 1024)
                   : "memory", "scc", EGC_LOOP_CLOBBERS);
    } else {
      xu = *(const u32x4*)(xup + ((u32)lane * 16u + (u32)wave * 1024u));
      xu24 = *(const u32*)(xup + ((u32)c * 16u + 3072u));
    }
  };

  {
    float zf = 0.f;
    asm volatile("" : "+v"(zf));
#pragma unroll
    for (int r = 0; r < 16; r++) acc[0][r] = acc[1][r] = zf;
  }
  int mx;
  bool spany;
  run_list(slot_round(0, true, mx, spany));

  if (mx > CAP || spany) {
    int base = CAP, spoff = 0, spw = 0;
    unsigned long long fm[4];
    {
      const int lane = fresh_lane();
#pragma unroll
      for (int k = 0; k < 4; k++) {
        const int ch = k * 64 + lane;
        fm[k] = __ballot(spany && ch < NCH && (*(const u32*)(ht + (ch < NCH ? ch : 0) * 8) & 0x80u) != 0u);
      }
    }
    for (;;) {
      int m;
      if (base < mx) {
        __syncthreads();
        int mx2;
        bool sp2;
        m = slot_round(base, false, mx2, sp2);
        base += CAP;
      } else {
        while (spw < 4 && (spw == 0 ? fm[0] : spw == 1 ? fm[1] : spw == 2 ? fm[2] : fm[3]) == 0ull) spw++;
        if (spw >= 4) break;
        const int spch = spw * 64 + __builtin_ctzll(spw == 0 ? fm[0] : spw == 1 ? fm[1] : spw == 2 ? fm[2] : fm[3]);
        __syncthreads();
        const int lane = fresh_lane(), tid = wave * 64 + lane;
        const int n = (int)((const u32*)(ws + WS_SPN))[spch];
        const u32x2* sl = (const u32x2*)(ws + WS_SPILL) + (size_t)spch * CH;
        u32 rr[RPT], ee[RPT], run = 0;
#pragma unroll
        for (int k = 0; k < RPT; k++) {
          const int idx = spoff + k * NT + tid;
          u32x2 v = {0xffffffffu, 0u};
          if (idx < n) v = sl[idx];
          const bool ok = idx < n && (v[0] & 511u) == (u32)b;
          rr[k] = ok ? v[1] : REC_NONE;
          ee[k] = (v[0] >> 9) * 32u;
          run += (u32)__popcll(__ballot(ok));
        }
        if (lane == 0) misc[wv] = run;
        __syncthreads();
        u32 wb = 0, tot = 0;
#pragma unroll
        for (int w = 0; w < NW; w++) {
          const u32 v = misc[sub * NW + w];
          wb += w < wave ? v : 0u;
          tot += v;
        }
        m = __builtin_amdgcn_readfirstlane((int)tot);
        run = wb;
#pragma unroll
        for (int k = 0; k < RPT; k++) {
          const unsigned long long mk = __ballot(rr[k] != REC_NONE);
          const u32 p = run + __builtin_amdgcn_mbcnt_hi((u32)(mk >> 32), __builtin_amdgcn_mbcnt_lo((u32)mk, 0u));
          run += (u32)__popcll(mk);
          if (rr[k] != REC_NONE) raw[p] = u32x2{rr[k], ee[k]};
        }
        spoff += CAP;
        if (spoff >= n) {
          spoff = 0;
#pragma unroll
          for (int k = 0; k < 4; k++) if (k == spw) fm[k] &= fm[k] - 1;
        }
      }
      run_list(m);
    }
  }

  const u32 lane2 = (u32)fresh_lane(), c2 = lane2 & 31u, hk2 = lane2 >> 5;
  char* rawb = lds_all + G_RED + sub * (NW * 2304);
  {
    char* pi = lds_all + G_IMG + wv * (2 * IMG) + lane2 * 16u;
    char* pr = rawb + wave * 2304 + lane2 * 16u;
#pragma unroll
    for (int t = 0; t < 6; t++) {
      const int q = t >> 1, p = t & 1;
      const f32x4 v = f32x4{acc[p][4 * q], acc[p][4 * q + 1], acc[p][4 * q + 2], acc[p][4 * q + 3]};
      if (t < 4) *(f32x4*)(pi + t * 1024) = v; else *(f32x4*)(pr + (t - 4) * 1024) = v;
    }
    if (hk2 == 0) *(f32x2*)(rawb + wave * 2304 + 2048 + lane2 * 8u) = f32x2{acc[0][12], acc[1][12]};
  }
  __syncthreads();
  {
    f32x4 y[2];
    f32x2 y24;
    const char* p0 = (wave < 2 ? lds_all + G_IMG + sub * (NW * 2 * IMG) + wave * 2048 : rawb) + lane2 * 16u;
    const int stp = wave < 2 ? 2 * IMG : 2304;
    y[0] = *(const f32x4*)p0;
    y[1] = *(const f32x4*)(p0 + 1024);
    y24 = *(const f32x2*)(rawb + 2048 + c2 * 8u);
#pragma unroll
    for (int w = 1; w < NW; w++) {
      y[0] += *(const f32x4*)(p0 + w * stp);
      y[1] += *(const f32x4*)(p0 + w * stp + 1024);
      y24 += *(const f32x2*)(rawb + w * 2304 + 2048 + c2 * 8u);
    }
    char* ob = (char*)out + (size_t)(b * BN) * (DM * 2) + 128 * g;
#pragma unroll
    for (int i = 0; i < 5; i++) {
      const u32 row = i < 4 ? i + 8u * wave + 4u * hk2 : 24u;
      const u32 xv = i < 4 ? xu[i] : xu24;
      const float y0 = (i < 4 ? y[0][i] : y24[0]) + __uint_as_float(xv << 16);
      const float y1 = (i < 4 ? y[1][i] : y24[1]) + __uint_as_float(xv & 0xffff0000u);
      const float o0 = y0 * __builtin_amdgcn_rcpf(1.f + __builtin_amdgcn_exp2f(y0 * NLOG2E));
      const float o1 = y1 * __builtin_amdgcn_rcpf(1.f + __builtin_amdgcn_exp2f(y1 * NLOG2E));
      if (i < 4 || (wave == 0 && hk2 == 0)) *(u32*)(ob + (row * 256u + c2 * 4u)) = cvt_pk_bf16(o0, o1);
    }
  }
}

}

extern "C" void kernel_launch(void* const* d_in, const int* in_sizes, int n_in, void* d_out, int out_size,
                              void* d_ws, size_t ws_size, hipStream_t stream) {
  const u16* x = (const u16*)d_in[0];
  const u16* ea = (const u16*)d_in[1];
  const int* src = (const int*)d_in[2];
  const int* dst = (const int*)d_in[3];
  const u16* uw = (const u16*)d_in[4];
  const u16* vw = (const u16*)d_in[5];
  const u16* gw = (const u16*)d_in[6];
  egc_prep<<<G1, 512, 0, stream>>>(x, ea, src, dst, uw, vw, gw, (char*)d_ws);
  egc_edge<<<2 * NSB, NTA, 0, stream>>>(gw, (const char*)d_ws, (u16*)d_out);
}
```
